```python
import torch

def _softmax(x, axis=-1):
    return torch.softmax(x, dim=axis)
def _gelu(x):
    return torch.nn.functional.gelu(x, approximate="tanh")
def _silu(x):
    return torch.nn.functional.silu(x)
def _sigmoid(x):
    return torch.sigmoid(x)
def _layernorm(x, axis=-1, eps=1e-5):
    m = x.mean(dim=axis, keepdim=True)
    v = x.var(dim=axis, keepdim=True, unbiased=False)
    return (x - m) / torch.sqrt(v + eps)
def _scatter_add(tgt, idx, src, axis=0):
    return tgt.clone().index_add_(axis, idx, src)
def _segment_sum(src, seg, n):
    out = torch.zeros((n,) + src.shape[1:], dtype=src.dtype)
    return out.index_add_(0, seg, src)
def _pad(x, pw):
    flat = tuple(v for pair in reversed(pw) for v in pair)
    return torch.nn.functional.pad(x, flat)

kernel_name = 'standalone__cross_network_dcn__recsys'

def setup_inputs():

    _w = (lambda a: (lambda t: t.to(torch.bfloat16) if t.is_floating_point() else t)(torch.as_tensor(a)))
    B, F, D, L = 16384, 26, 16, 6
    Din = min(F * D, 1024)
    return {
        "x0": _w((lambda *s: torch.randn(*s, dtype=torch.bfloat16) * 0.05)(B, Din)),
        "ws": _w((lambda *s: torch.randn(*s, dtype=torch.bfloat16) * 0.8)(L, Din, Din) * (Din ** -0.5)),
        "bs": _w((lambda *s: torch.randn(*s, dtype=torch.bfloat16) * 3.2)(L, Din) * 0.01),
    }

def reference(x0, ws, bs):
    x = x0
    L = ws.shape[0]
    for l in range(L):
        proj = torch.einsum('bd,de->be', x, ws[l]) + bs[l]
        x = x0 * proj + x
    return x
```

```cpp
#include <hip/hip_runtime.h>
#include <hip/hip_bf16.h>
#include <stdint.h>

typedef __attribute__((ext_vector_type(8))) __bf16 bf16x8;
typedef __attribute__((ext_vector_type(4))) __bf16 bf16x4;
typedef __attribute__((ext_vector_type(2))) __bf16 bf16x2;
typedef __attribute__((ext_vector_type(4))) float f32x4;
typedef __attribute__((ext_vector_type(2))) float f32x2;
typedef __attribute__((ext_vector_type(2))) unsigned u32x2;
typedef __attribute__((ext_vector_type(4))) unsigned u32x4;

#define LAS __attribute__((address_space(3)))
#define DI __device__ __forceinline__
#define SB() __builtin_amdgcn_sched_barrier(0)
#define MEMFENCE() asm volatile("" ::: "memory")

constexpr int DF = 416;
constexpr int NL = 6;
constexpr int KS = 13;
constexpr int ROWB = DF * 2;
constexpr int BM = 64;
constexpr int STAGE = 32 * ROWB;
constexpr int XIMG = 0;
constexpr int XIMG_SZ = BM * ROWB;
constexpr int RING = XIMG + XIMG_SZ;
constexpr int NSLOT = 4;
constexpr int BIASO = RING + NSLOT * STAGE;
constexpr int LDS_TOTAL = BIASO + 2048;
constexpr int WBYTES = NL * DF * ROWB;

enum { FIRST = 0, MID = 1, LAST = 2 };

constexpr int waitimm(int vm, int lgkm) { return (vm & 15) | (7 << 4) | ((lgkm & 15) << 8) | ((vm >> 4) << 14); }

template <int MODE> constexpr int vm_cnt(int j) {
  if (j < 0) return MODE == FIRST ? 8 : 7;
  if (MODE == LAST) return j + 4 < KS ? 7 : 0;
  if (MODE == FIRST) return 7 + (j + 4 < KS ? 1 : 0);
  return 7;
}

struct Ctx {
  unsigned wbase_e, wbase_o, wbase_s;
  unsigned xrd;
  unsigned xwr_e, xwr_o, xwr_s;
  unsigned brd, brd_s;
  unsigned voff[7];
  unsigned xvoff;
  unsigned bvoff;
  unsigned m0w;
  unsigned m0x;
  unsigned m0b;
  unsigned lds0;
};

DI bf16x4 tr_read(unsigned addr) {
  return __builtin_amdgcn_ds_read_tr16_b64_v4bf16((LAS bf16x4*)(size_t)addr);
}
DI bf16x8 wfrag(unsigned base, int imm) {
  bf16x4 lo = tr_read(base + imm);
  bf16x4 hi = tr_read(base + imm + 4 * ROWB);
  return __builtin_shufflevector(lo, hi, 0, 1, 2, 3, 4, 5, 6, 7);
}
DI bf16x8 lds_b128(unsigned addr) { return *(LAS bf16x8*)(size_t)addr; }
DI u32x2 lds_b64(unsigned addr) { return *(LAS u32x2*)(size_t)addr; }
DI void lds_w64(unsigned addr, u32x2 v) { *(LAS u32x2*)(size_t)addr = v; }

DI void set_m0(unsigned v) { asm volatile("s_mov_b32 m0, %0" ::"s"(v) : "memory"); }
template <int IMM>
DI void dma16(unsigned voff, __amdgpu_buffer_rsrc_t rsrc, unsigned soff) {
  asm volatile("buffer_load_dwordx4 %0, %1, %2 offen offset:%3 lds" ::"v"(voff), "s"(rsrc), "s"(soff), "i"(IMM) : "memory");
}
template <int IMM>
DI void dma16_masked(unsigned voff, __amdgpu_buffer_rsrc_t rsrc, unsigned soff, unsigned long long mask) {
  asm volatile(
      "s_mov_b64 exec, %4\n\t"
      "buffer_load_dwordx4 %0, %1, %2 offen offset:%3 lds\n\t"
      "s_mov_b64 exec, -1" ::"v"(voff), "s"(rsrc), "s"(soff), "i"(IMM), "s"(mask)
      : "memory");
}

DI unsigned pack2(float lo, float hi) {
  f32x2 v = {lo, hi};
  bf16x2 b = __builtin_convertvector(v, bf16x2);
  return __builtin_bit_cast(unsigned, b);
}
DI f32x4 unpack4(u32x2 p) {
  f32x4 r;
  r[0] = __builtin_bit_cast(float, p[0] << 16);
  r[1] = __builtin_bit_cast(float, p[0] & 0xffff0000u);
  r[2] = __builtin_bit_cast(float, p[1] << 16);
  r[3] = __builtin_bit_cast(float, p[1] & 0xffff0000u);
  return r;
}

#define MFMA(a, b, c) __builtin_amdgcn_mfma_f32_16x16x32_bf16((a), (b), (c), 0, 0, 0)

struct State {
  f32x4 acc[26];
  f32x4 x0[26];
  f32x4 x[26];
  bf16x8 wf[7];
  bf16x8 xf[2][4];
  f32x4 bias[7];
};

template <int MODE, int S>
DI void kstep(const Ctx& c, State& st, const __amdgpu_buffer_rsrc_t wsrd, const __amdgpu_buffer_rsrc_t xsrd,
              const __amdgpu_buffer_rsrc_t bsrd, const int layer, const int btsh) {
  constexpr int P = S & 1;
  constexpr bool HAS_NEXT_W = !(MODE == LAST && S == KS - 1);
  constexpr bool HAS_NEXT_X = (S < KS - 1);
  constexpr bool DO_DMA = !(MODE == LAST && S + 4 >= KS);
  const int gs = layer * KS + S;
  const unsigned slot_n = (unsigned)((gs + 1) & 3) * STAGE;
  const unsigned slot_d = (unsigned)(gs & 3) * STAGE;
  const unsigned soff = (unsigned)(gs + 4) * STAGE;

#define MM(ft, bt)                                                                    \
  do {                                                                                \
    if constexpr (S == 0) st.acc[(ft)*4 + (bt)] = MFMA(st.wf[ft], st.xf[P][bt], st.bias[ft]); \
    else st.acc[(ft)*4 + (bt)] = MFMA(st.wf[ft], st.xf[P][bt], st.acc[(ft)*4 + (bt)]);  \
  } while (0)

  unsigned be = 0, bo = 0, bs = 0;
  MM(0, 0); if constexpr (HAS_NEXT_W) be = c.wbase_e + slot_n; SB();
  MM(0, 1); if constexpr (HAS_NEXT_W) bo = c.wbase_o + slot_n; SB();
  MM(0, 2); if constexpr (HAS_NEXT_W) bs = c.wbase_s + slot_n; SB();
  MM(0, 3); SB();
  MM(1, 0); SB();
  MM(1, 1); SB();
  MM(1, 2); SB();
  MM(1, 3); SB();
  MEMFENCE();
  __builtin_amdgcn_s_waitcnt(waitimm(vm_cnt<MODE>(S - 2) + vm_cnt<MODE>(S - 1), 0));
  __builtin_amdgcn_s_barrier();
  MEMFENCE();
  SB();
  MM(2, 0); if constexpr (HAS_NEXT_W) st.wf[0] = wfrag(be, 0); SB();
  MM(2, 1); if constexpr (HAS_NEXT_W) st.wf[1] = wfrag(bo, 32); SB();
  MM(2, 2);
  if constexpr (HAS_NEXT_X) st.xf[P ^ 1][0] = lds_b128(c.xrd + (S + 1) * 4096 + 0 * 1024);
  if constexpr (DO_DMA) set_m0(c.m0w + slot_d);
  SB();
  MM(2, 3);
  if constexpr (DO_DMA) dma16<0>(c.voff[0], wsrd, soff);
  if constexpr (HAS_NEXT_X) st.xf[P ^ 1][1] = lds_b128(c.xrd + (S + 1) * 4096 + 1 * 1024);
  SB();
  MM(3, 0); if constexpr (HAS_NEXT_W) st.wf[2] = wfrag(be, 64); SB();
  MM(3, 1);
  if constexpr (DO_DMA) dma16<1024>(c.voff[1], wsrd, soff);
  if constexpr (HAS_NEXT_X) st.xf[P ^ 1][2] = lds_b128(c.xrd + (S + 1) * 4096 + 2 * 1024);
  SB();
  MM(3, 2);
  if constexpr (DO_DMA) dma16<2048>(c.voff[2], wsrd, soff);
  if constexpr (HAS_NEXT_X) st.xf[P ^ 1][3] = lds_b128(c.xrd + (S + 1) * 4096 + 3 * 1024);
  SB();
  MM(3, 3);
  if constexpr (DO_DMA) { dma16<3072>(c.voff[3], wsrd, soff); set_m0(c.m0w + slot_d + 4096); }
  SB();
  MM(4, 0); if constexpr (HAS_NEXT_W) st.wf[3] = wfrag(bo, 96); SB();
  MM(4, 1); if constexpr (DO_DMA) dma16<0>(c.voff[4], wsrd, soff); SB();
  MM(4, 2); if constexpr (DO_DMA) dma16<1024>(c.voff[5], wsrd, soff); SB();
  MM(4, 3); if constexpr (DO_DMA) dma16_masked<2048>(c.voff[6], wsrd, soff, 0xffffffffull); SB();
  MM(5, 0); if constexpr (HAS_NEXT_W) st.wf[4] = wfrag(be, 128); SB();
  MM(5, 1);
  if constexpr (MODE == FIRST && S + 4 < KS) {
    set_m0(c.m0x + (S + 4) * 4096);
  } else if constexpr (MODE != LAST && S == 10) {
    set_m0(c.m0b + (unsigned)((layer + 1) & 1) * 1024);
  }
  SB();
  MM(5, 2);
  if constexpr (MODE == FIRST && S + 4 < KS) {
    dma16<0>(c.xvoff, xsrd, (unsigned)((S + 4) * 64));
  } else if constexpr (MODE != LAST && S == 10) {
    dma16_masked<0>(c.bvoff, bsrd, (unsigned)(layer + 1) * ROWB, 0x1fffull);
  }
  SB();
  MM(5, 3); SB();
  {
    bf16x8 xa = btsh ? st.xf[P][2] : st.xf[P][0];
    bf16x8 xb = btsh ? st.xf[P][3] : st.xf[P][1];
    if constexpr (S == 0) st.acc[24] = MFMA(st.wf[6], xa, st.bias[6]);
    else st.acc[24] = MFMA(st.wf[6], xa, st.acc[24]);
    if constexpr (HAS_NEXT_W) st.wf[5] = wfrag(bo, 160);
    SB();
    if constexpr (S == 0) st.acc[25] = MFMA(st.wf[6], xb, st.bias[6]);
    else st.acc[25] = MFMA(st.wf[6], xb, st.acc[25]);
    if constexpr (HAS_NEXT_W) st.wf[6] = wfrag(bs, 0);
    SB();
  }
#undef MM
}

DI unsigned tile_addr(const Ctx& c, int t) {
  if (t < 24) {
    const int ft = t >> 2, bt = t & 3;
    return ((ft & 1) ? c.xwr_o : c.xwr_e) + (ft >> 1) * 4096 + bt * 1024;
  }
  return c.xwr_s + (t - 24) * 1024;
}

template <int MODE, int... Ss>
DI void layer_steps(const Ctx& c, State& st, const __amdgpu_buffer_rsrc_t wsrd, const __amdgpu_buffer_rsrc_t xsrd,
                    const __amdgpu_buffer_rsrc_t bsrd, const int layer, const int btsh,
                    std::integer_sequence<int, Ss...>) {
  (kstep<MODE, Ss>(c, st, wsrd, xsrd, bsrd, layer, btsh), ...);
}

DI void layer_head(const Ctx& c, State& st, const int layer) {
#pragma unroll
  for (int bt = 0; bt < 4; ++bt) st.xf[0][bt] = lds_b128(c.xrd + bt * 1024);
  const unsigned bo = (unsigned)(layer & 1) * 1024;
#pragma unroll
  for (int ft = 0; ft < 6; ++ft) st.bias[ft] = unpack4(lds_b64(c.brd + bo + ft * 32));
  st.bias[6] = unpack4(lds_b64(c.brd_s + bo));
}

template <int MODE>
DI void layer(const Ctx& c, State& st, const __amdgpu_buffer_rsrc_t wsrd, const __amdgpu_buffer_rsrc_t xsrd,
              const __amdgpu_buffer_rsrc_t bsrd, const int layer_i, const int btsh) {
  layer_head(c, st, layer_i);
  SB();
  layer_steps<MODE>(c, st, wsrd, xsrd, bsrd, layer_i, btsh, std::make_integer_sequence<int, KS>{});
  if constexpr (MODE == FIRST) {
#pragma unroll
    for (int t = 0; t < 26; ++t) {
      st.x0[t] = unpack4(lds_b64(tile_addr(c, t)));
      st.x[t] = st.x0[t];
    }
  }
#pragma unroll
  for (int t = 0; t < 26; ++t) {
#pragma unroll
    for (int i = 0; i < 4; ++i) st.x[t][i] = __builtin_fmaf(st.x0[t][i], st.acc[t][i], st.x[t][i]);
    if constexpr (MODE != LAST) {
      u32x2 p = {pack2(st.x[t][0], st.x[t][1]), pack2(st.x[t][2], st.x[t][3])};
      lds_w64(tile_addr(c, t), p);
    }
  }
  if constexpr (MODE != LAST) {
    MEMFENCE();
    __builtin_amdgcn_s_waitcnt(waitimm(63, 0));
    __builtin_amdgcn_s_barrier();
    MEMFENCE();
    SB();
  }
}

__global__ __launch_bounds__(256, 1) void dcn_fused_kernel(const __hip_bfloat16* __restrict__ x0g,
                                                            const __hip_bfloat16* __restrict__ wg,
                                                            const __hip_bfloat16* __restrict__ bg,
                                                            __hip_bfloat16* __restrict__ outg) {
  __shared__ __attribute__((aligned(1024))) char smem[LDS_TOTAL];
  const unsigned lds0 = (unsigned)(size_t)(LAS char*)smem;
  const int tid = threadIdx.x;
  const int w = __builtin_amdgcn_readfirstlane(tid >> 6);
  const int lane = tid & 63;
  const int cc = lane & 15, g = lane >> 4, q = (lane >> 2) & 3, p4 = lane & 3;
  const int row0 = blockIdx.x * BM;
  const int ftsh = 24 + (w >> 1);
  const int btsh = (w & 1) * 2;

  Ctx c;
  c.lds0 = lds0;
  {
    const unsigned rb = lds0 + RING + (unsigned)(8 * g + q) * ROWB + 8 * p4;
    const unsigned sw = 32u * (g & 1);
    c.wbase_e = rb + sw + 32u * (6 * w);
    c.wbase_o = rb - sw + 32u * (6 * w);
    c.wbase_s = ((ftsh & 1) ? rb - sw : rb + sw) + 32u * ftsh;
    const unsigned h = (cc >> 1) & 3;
    c.xrd = lds0 + XIMG + cc * 64 + ((g ^ h) * 16);
    const unsigned ae = cc * 64 + ((((g >> 1)) ^ h) * 16) + 8 * (g & 1);
    const unsigned ao = cc * 64 + (((2 + (g >> 1)) ^ h) * 16) + 8 * (g & 1);
    c.xwr_e = lds0 + XIMG + ae + (3 * w) * 4096;
    c.xwr_o = lds0 + XIMG + ao + (3 * w) * 4096;
    c.xwr_s = lds0 + XIMG + ((ftsh & 1) ? ao : ae) + 12 * 4096 + btsh * 1024;
    c.brd = lds0 + BIASO + (6 * w) * 32 + 8 * g;
    c.brd_s = lds0 + BIASO + ftsh * 32 + 8 * g;
#pragma unroll
    for (int j = 0; j < 7; ++j) {
      int p = w * 416 + j * 64 + ((j == 6) ? (lane & 31) : lane);
      int r = p / 52, cp = p - r * 52;
      int sc = cp ^ (2 * ((r >> 3) & 1));
      c.voff[j] = (unsigned)(r * ROWB + sc * 16 - (j & 3) * 1024);
    }
    {
      const int cr = lane >> 2, ch = lane & 3;
      c.xvoff = (unsigned)((16 * w + cr) * ROWB + ((ch ^ ((cr >> 1) & 3)) * 16));
    }
    c.bvoff = (unsigned)(w * 208 + (lane < 13 ? lane : 0) * 16);
    c.m0w = lds0 + RING + w * 6656;
    c.m0x = lds0 + XIMG + w * 1024;
    c.m0b = lds0 + BIASO + w * 208;
  }
  const __amdgpu_buffer_rsrc_t wsrd = __builtin_amdgcn_make_buffer_rsrc((void*)wg, 0, WBYTES, 0x00020000);
  const __amdgpu_buffer_rsrc_t xsrd =
      __builtin_amdgcn_make_buffer_rsrc((void*)(x0g + (size_t)row0 * DF), 0, BM * ROWB, 0x00020000);
  const __amdgpu_buffer_rsrc_t bsrd = __builtin_amdgcn_make_buffer_rsrc((void*)bg, 0, NL * ROWB, 0x00020000);

  asm volatile("s_nop 4" ::: "memory");
  set_m0(c.m0b);
  asm volatile("s_nop 0");
  dma16_masked<0>(c.bvoff, bsrd, 0u, 0x1fffull);
#pragma unroll
  for (int j = 0; j < 4; ++j) {
    set_m0(c.m0x + j * 4096);
    asm volatile("s_nop 0");
    dma16<0>(c.xvoff, xsrd, (unsigned)(j * 64));
    set_m0(c.m0w + j * STAGE);
    asm volatile("s_nop 0");
    dma16<0>(c.voff[0], wsrd, (unsigned)(j * STAGE));
    dma16<1024>(c.voff[1], wsrd, (unsigned)(j * STAGE));
    dma16<2048>(c.voff[2], wsrd, (unsigned)(j * STAGE));
    dma16<3072>(c.voff[3], wsrd, (unsigned)(j * STAGE));
    set_m0(c.m0w + j * STAGE + 4096);
    asm volatile("s_nop 0");
    dma16<0>(c.voff[4], wsrd, (unsigned)(j * STAGE));
    dma16<1024>(c.voff[5], wsrd, (unsigned)(j * STAGE));
    dma16_masked<2048>(c.voff[6], wsrd, (unsigned)(j * STAGE), 0xffffffffull);
  }
  MEMFENCE();
  __builtin_amdgcn_s_waitcnt(waitimm(24, 15));
  __builtin_amdgcn_s_barrier();
  MEMFENCE();
  SB();

  State st;
  st.wf[0] = wfrag(c.wbase_e, 0);
  st.wf[1] = wfrag(c.wbase_o, 32);
  st.wf[2] = wfrag(c.wbase_e, 64);
  st.wf[3] = wfrag(c.wbase_o, 96);
  st.wf[4] = wfrag(c.wbase_e, 128);
  st.wf[5] = wfrag(c.wbase_o, 160);
  st.wf[6] = wfrag(c.wbase_s, 0);

  layer<FIRST>(c, st, wsrd, xsrd, bsrd, 0, btsh);
#pragma unroll 1
  for (int l = 1; l < NL - 1; ++l) layer<MID>(c, st, wsrd, xsrd, bsrd, l, btsh);
  layer<LAST>(c, st, wsrd, xsrd, bsrd, NL - 1, btsh);

  char* ob = (char*)(outg + (size_t)row0 * DF);
  {
    const unsigned obase = (unsigned)(cc * ROWB + (g >> 1) * 16);
#pragma unroll
    for (int fp = 0; fp < 3; ++fp) {
#pragma unroll
      for (int bt = 0; bt < 4; ++bt) {
        const f32x4 a = st.x[(2 * fp) * 4 + bt], b = st.x[(2 * fp + 1) * 4 + bt];
        unsigned a0 = pack2(a[0], a[1]), a1 = pack2(a[2], a[3]);
        unsigned b0 = pack2(b[0], b[1]), b1 = pack2(b[2], b[3]);
        auto r0 = __builtin_amdgcn_permlane16_swap(a0, b0, false, false);
        auto r1 = __builtin_amdgcn_permlane16_swap(a1, b1, false, false);
        u32x4 v = {r0[0], r1[0], r0[1], r1[1]};
        const unsigned off = obase + (unsigned)(bt * 16 * ROWB + (6 * w + 2 * fp) * 32) + (g & 1) * 32;
        *(u32x4*)(ob + off) = v;
      }
    }
    {
      const f32x4 a = st.x[24], b = st.x[25];
      unsigned a0 = pack2(a[0], a[1]), a1 = pack2(a[2], a[3]);
      unsigned b0 = pack2(b[0], b[1]), b1 = pack2(b[2], b[3]);
      auto r0 = __builtin_amdgcn_permlane16_swap(a0, b0, false, false);
      auto r1 = __builtin_amdgcn_permlane16_swap(a1, b1, false, false);
      u32x4 v = {r0[0], r1[0], r0[1], r1[1]};
      const unsigned off = obase + (unsigned)((btsh + (g & 1)) * 16 * ROWB + ftsh * 32);
      *(u32x4*)(ob + off) = v;
    }
  }
}

extern "C" void kernel_launch(void* const* d_in, const int* in_sizes, int n_in, void* d_out, int out_size,
                              void* d_ws, size_t ws_size, hipStream_t stream) {
  const __hip_bfloat16* x0 = (const __hip_bfloat16*)d_in[0];
  const __hip_bfloat16* ws = (const __hip_bfloat16*)d_in[1];
  const __hip_bfloat16* bs = (const __hip_bfloat16*)d_in[2];
  __hip_bfloat16* out = (__hip_bfloat16*)d_out;
  const int B = in_sizes[0] / DF;
  dcn_fused_kernel<<<B / BM, 256, 0, stream>>>(x0, ws, bs, out);
}
```

```cpp
#include <hip/hip_runtime.h>
#include <hip/hip_bf16.h>
#include <stdint.h>

typedef __attribute__((ext_vector_type(8))) __bf16 bf16x8;
typedef __attribute__((ext_vector_type(4))) __bf16 bf16x4;
typedef __attribute__((ext_vector_type(2))) __bf16 bf16x2;
typedef __attribute__((ext_vector_type(4))) float f32x4;
typedef __attribute__((ext_vector_type(2))) float f32x2;
typedef __attribute__((ext_vector_type(2))) unsigned u32x2;
typedef __attribute__((ext_vector_type(4))) unsigned u32x4;

#define LAS __attribute__((address_space(3)))
#define DI __device__ __forceinline__
#define SB() __builtin_amdgcn_sched_barrier(0)
#define MEMFENCE() asm volatile("" ::: "memory")

constexpr int DF = 416;
constexpr int NL = 6;
constexpr int KS = 13;
constexpr int ROWB = DF * 2;
constexpr int BM = 64;
constexpr int STAGE = 32 * ROWB;
constexpr int XIMG = 0;
constexpr int XIMG_SZ = BM * ROWB;
constexpr int RING = XIMG + XIMG_SZ;
constexpr int NSLOT = 4;
constexpr int BIASO = RING + NSLOT * STAGE;
constexpr int LDS_TOTAL = BIASO + 2048;
constexpr int WBYTES = NL * DF * ROWB;

constexpr int waitimm(int vm, int lgkm) { return (vm & 15) | (7 << 4) | ((lgkm & 15) << 8) | ((vm >> 4) << 14); }

struct Ctx {
  unsigned wbase_e, wbase_o, wbase_s;
  unsigned xrdA, xrdB;
  unsigned xwr_eA, xwr_oA, xwr_eB, xwr_oB, xwr_s;
  unsigned brd, brd_s;
  unsigned voffA, voffB;
  unsigned xvoff;
  unsigned bvoff;
  unsigned m0w;
  unsigned m0x;
  unsigned m0b;
};

DI bf16x4 tr_read(unsigned addr) {
  return __builtin_amdgcn_ds_read_tr16_b64_v4bf16((LAS bf16x4*)(size_t)addr);
}
DI bf16x8 wfrag(unsigned base, int imm) {
  bf16x4 lo = tr_read(base + imm);
  bf16x4 hi = tr_read(base + imm + 4 * ROWB);
  return __builtin_shufflevector(lo, hi, 0, 1, 2, 3, 4, 5, 6, 7);
}
DI bf16x8 lds_b128(unsigned addr) { return *(LAS bf16x8*)(size_t)addr; }
DI u32x4 lds_u128(unsigned addr) { return *(LAS u32x4*)(size_t)addr; }
DI u32x2 lds_b64(unsigned addr) { return *(LAS u32x2*)(size_t)addr; }
DI void lds_w64(unsigned addr, u32x2 v) { *(LAS u32x2*)(size_t)addr = v; }

DI void set_m0(unsigned v) { asm volatile("s_mov_b32 m0, %0" ::"s"(v) : "memory"); }
template <int IMM>
DI void dma16(unsigned voff, __amdgpu_buffer_rsrc_t rsrc, unsigned soff) {
  asm volatile("buffer_load_dwordx4 %0, %1, %2 offen offset:%3 lds" ::"v"(voff), "s"(rsrc), "s"(soff), "i"(IMM) : "memory");
}
template <int IMM>
DI void dma16_masked(unsigned voff, __amdgpu_buffer_rsrc_t rsrc, unsigned soff, unsigned long long mask) {
  asm volatile(
      "s_mov_b64 exec, %4\n\t"
      "buffer_load_dwordx4 %0, %1, %2 offen offset:%3 lds\n\t"
      "s_mov_b64 exec, -1" ::"v"(voff), "s"(rsrc), "s"(soff), "i"(IMM), "s"(mask)
      : "memory");
}

DI unsigned pack2(float lo, float hi) {
  f32x2 v = {lo, hi};
  bf16x2 b = __builtin_convertvector(v, bf16x2);
  return __builtin_bit_cast(unsigned, b);
}
DI f32x4 unpack4(u32x2 p) {
  f32x4 r;
  r[0] = __builtin_bit_cast(float, p[0] << 16);
  r[1] = __builtin_bit_cast(float, p[0] & 0xffff0000u);
  r[2] = __builtin_bit_cast(float, p[1] << 16);
  r[3] = __builtin_bit_cast(float, p[1] & 0xffff0000u);
  return r;
}

#define MFMA(a, b, c) __builtin_amdgcn_mfma_f32_16x16x32_bf16((a), (b), (c), 0, 0, 0)

struct State {
  f32x4 acc[26];
  f32x4 x0[26];
  f32x4 x[26];
  bf16x8 wf[7];
  bf16x8 xf[4];
  f32x4 bias[7];
  unsigned xa, xb;
  unsigned slot_n;
  unsigned slot_d;
  unsigned soff;
  int gs;
};

template <bool FIRSTK>
DI void kstep(const Ctx& c, State& st, const __amdgpu_buffer_rsrc_t wsrd, const __amdgpu_buffer_rsrc_t xsrd,
              const __amdgpu_buffer_rsrc_t bsrd, const int layer) {
#define MM(ft, bt)                                                                             \
  do {                                                                                         \
    constexpr int T_ = (ft) < 6 ? (ft)*4 + (bt) : 24 + (bt);                                    \
    if constexpr (FIRSTK) st.acc[T_] = MFMA(st.wf[ft], st.xf[bt], st.bias[ft]);                \
    else st.acc[T_] = MFMA(st.wf[ft], st.xf[bt], st.acc[T_]);                                  \
  } while (0)

  MM(0, 0); st.xf[2] = lds_b128(st.xb); SB();
  MM(0, 1); st.xf[3] = lds_b128(st.xb + 1024); SB();
  MM(1, 0); const unsigned be = c.wbase_e + st.slot_n; const unsigned bo = c.wbase_o + st.slot_n; SB();
  MM(1, 1); const unsigned bs = c.wbase_s + st.slot_n; st.xa += 4096; SB();
  MM(2, 0); st.xb += 4096; SB();
  MM(2, 1); SB();
  MM(3, 0); SB();
  MM(3, 1); SB();
  MEMFENCE();
  __builtin_amdgcn_s_waitcnt(waitimm(14, 0));
  __builtin_amdgcn_s_barrier();
  MEMFENCE();
  SB();
  MM(4, 0); set_m0(c.m0w + st.slot_d); SB();
  MM(4, 1); dma16<0>(c.voffA, wsrd, st.soff); SB();
  MM(5, 0); dma16<1024>(c.voffA, wsrd, st.soff); SB();
  MM(5, 1); dma16<2048>(c.voffA, wsrd, st.soff); SB();
  MM(6, 0); dma16<3072>(c.voffA, wsrd, st.soff); set_m0(c.m0w + st.slot_d + 4096); SB();
  MM(6, 1); dma16<0>(c.voffB, wsrd, st.soff); SB();
  MM(0, 2); st.xf[0] = lds_b128(st.xa); st.xf[1] = lds_b128(st.xa + 1024); SB();
  MM(0, 3); st.wf[6] = wfrag(bs, 0); SB();
  MM(1, 2); st.wf[0] = wfrag(be, 0); SB();
  MM(1, 3); dma16<1024>(c.voffB, wsrd, st.soff); SB();
  MM(2, 2); st.wf[1] = wfrag(bo, 32); SB();
  MM(2, 3); dma16_masked<2048>(c.voffB, wsrd, st.soff, 0xffffffffull); SB();
  MM(3, 2); st.wf[2] = wfrag(be, 64); SB();
  MM(3, 3);
  if (st.gs + 4 < KS) {
    set_m0(c.m0x + (unsigned)(st.gs + 4) * 4096);
    asm volatile("s_nop 0");
    dma16<0>(c.xvoff, xsrd, (unsigned)(st.gs + 4) * 64);
  }
  SB();
  MM(4, 2); st.wf[3] = wfrag(bo, 96); SB();
  MM(4, 3);
  if constexpr (FIRSTK) {
    set_m0(c.m0b + (unsigned)((layer + 1) & 1) * 1024);
    asm volatile("s_nop 0");
    dma16_masked<0>(c.bvoff, bsrd, (unsigned)(layer + 1) * ROWB, 0x1fffull);
  }
  SB();
  MM(5, 2); st.wf[4] = wfrag(be, 128); SB();
  MM(5, 3);
  st.slot_d = st.slot_n;
  st.slot_n = (st.slot_n == (NSLOT - 1) * STAGE) ? 0u : st.slot_n + STAGE;
  st.soff += STAGE;
  st.gs += 1;
  SB();
  st.wf[5] = wfrag(bo, 160);
  SB();
#undef MM
}

DI unsigned tile_addr(const Ctx& c, int t) {
  if (t < 24) {
    const int ft = t >> 2, bt = t & 3;
    const unsigned b = (bt < 2) ? ((ft & 1) ? c.xwr_oA : c.xwr_eA) : ((ft & 1) ? c.xwr_oB : c.xwr_eB);
    return b + (ft >> 1) * 4096 + (bt & 1) * 1024;
  }
  return c.xwr_s + (t - 24) * 1024;
}

__global__ __launch_bounds__(256, 1) void dcn_fused_kernel(const __hip_bfloat16* __restrict__ x0g,
                                                            const __hip_bfloat16* __restrict__ wg,
                                                            const __hip_bfloat16* __restrict__ bg,
                                                            __hip_bfloat16* __restrict__ outg) {
  __shared__ __attribute__((aligned(4096))) char smem[LDS_TOTAL];
  const unsigned lds0 = (unsigned)(size_t)(LAS char*)smem;
  const int tid = threadIdx.x;
  const int w = __builtin_amdgcn_readfirstlane(tid >> 6);
  const int lane = tid & 63;
  const int row0 = blockIdx.x * BM;

  const __amdgpu_buffer_rsrc_t wsrd = __builtin_amdgcn_make_buffer_rsrc((void*)wg, 0, WBYTES, 0x00020000);
  const __amdgpu_buffer_rsrc_t xsrd =
      __builtin_amdgcn_make_buffer_rsrc((void*)(x0g + (size_t)row0 * DF), 0, BM * ROWB, 0x00020000);
  const __amdgpu_buffer_rsrc_t bsrd = __builtin_amdgcn_make_buffer_rsrc((void*)bg, 0, NL * ROWB, 0x00020000);

  Ctx c;
  c.voffA = (unsigned)(w * 6656 + ((lane ^ (2 * (w & 1))) * 16));
  c.voffB = c.voffA + 4096;
  {
    const int cr = lane >> 2, ch = lane & 3;
    c.xvoff = (unsigned)((16 * w + cr) * ROWB + ((ch ^ ((cr >> 1) & 3)) * 16));
  }
  c.bvoff = (unsigned)(w * 208 + (lane < 13 ? lane : 0) * 16);
  c.m0w = lds0 + RING + w * 6656;
  c.m0x = lds0 + XIMG + w * 1024;
  c.m0b = lds0 + BIASO + w * 208;

  asm volatile("s_nop 4" ::: "memory");
  set_m0(c.m0b);
  asm volatile("s_nop 0");
  dma16_masked<0>(c.bvoff, bsrd, 0u, 0x1fffull);
#pragma unroll 1
  for (int j = 0; j < 4; ++j) {
    const unsigned so = (unsigned)(j * STAGE);
    set_m0(c.m0x + j * 4096);
    asm volatile("s_nop 0");
    dma16<0>(c.xvoff, xsrd, (unsigned)(j * 64));
    set_m0(c.m0w + so);
    asm volatile("s_nop 0");
    dma16<0>(c.voffA, wsrd, so);
    dma16<1024>(c.voffA, wsrd, so);
    dma16<2048>(c.voffA, wsrd, so);
    dma16<3072>(c.voffA, wsrd, so);
    set_m0(c.m0w + so + 4096);
    asm volatile("s_nop 0");
    dma16<0>(c.voffB, wsrd, so);
    dma16<1024>(c.voffB, wsrd, so);
    dma16_masked<2048>(c.voffB, wsrd, so, 0xffffffffull);
  }
  SB();

  const int cc = lane & 15, g = lane >> 4, q = (lane >> 2) & 3, p4 = lane & 3;
  const int ftsh = 24 + (w >> 1);
  const int btsh = (w & 1) * 2;
  {
    const unsigned rb = lds0 + RING + (unsigned)(8 * g + q) * ROWB + 8 * p4;
    const unsigned sw = 32u * (g & 1);
    c.wbase_e = rb + sw + 32u * (6 * w);
    c.wbase_o = rb - sw + 32u * (6 * w);
    c.wbase_s = ((ftsh & 1) ? rb - sw : rb + sw) + 32u * ftsh;
    const unsigned h = (cc >> 1) & 3;
    const unsigned xr = lds0 + XIMG + cc * 64 + ((g ^ h) * 16);
    c.xrdA = xr + btsh * 1024;
    c.xrdB = xr + (2 - btsh) * 1024;
    const unsigned ae = lds0 + XIMG + cc * 64 + ((((g >> 1)) ^ h) * 16) + 8 * (g & 1);
    const unsigned ao = lds0 + XIMG + cc * 64 + (((2 + (g >> 1)) ^ h) * 16) + 8 * (g & 1);
    c.xwr_eA = ae + (3 * w) * 4096 + btsh * 1024;
    c.xwr_oA = ao + (3 * w) * 4096 + btsh * 1024;
    c.xwr_eB = ae + (3 * w) * 4096 + (2 - btsh) * 1024;
    c.xwr_oB = ao + (3 * w) * 4096 + (2 - btsh) * 1024;
    c.xwr_s = ((ftsh & 1) ? ao : ae) + 12 * 4096 + btsh * 1024;
    c.brd = lds0 + BIASO + (6 * w) * 32 + 8 * g;
    c.brd_s = lds0 + BIASO + ftsh * 32 + 8 * g;
  }

  MEMFENCE();
  __builtin_amdgcn_s_waitcnt(waitimm(24, 15));
  __builtin_amdgcn_s_barrier();
  MEMFENCE();
  SB();

  State st;
  st.wf[0] = wfrag(c.wbase_e, 0);
  st.wf[1] = wfrag(c.wbase_o, 32);
  st.wf[2] = wfrag(c.wbase_e, 64);
  st.wf[3] = wfrag(c.wbase_o, 96);
  st.wf[4] = wfrag(c.wbase_e, 128);
  st.wf[5] = wfrag(c.wbase_o, 160);
  st.wf[6] = wfrag(c.wbase_s, 0);
  st.slot_d = 0;
  st.slot_n = STAGE;
  st.soff = 4 * STAGE;
  st.gs = 0;
#pragma unroll
  for (int t = 0; t < 26; ++t) {
    st.x0[t] = f32x4{0.f, 0.f, 0.f, 0.f};
    st.x[t] = f32x4{0.f, 0.f, 0.f, 0.f};
  }

#pragma unroll 1
  for (int l = 0; l < NL; ++l) {
    st.xa = c.xrdA;
    st.xb = c.xrdB;
    st.xf[0] = lds_b128(st.xa);
    st.xf[1] = lds_b128(st.xa + 1024);
    {
      const unsigned bo = (unsigned)(l & 1) * 1024;
#pragma unroll
      for (int ft = 0; ft < 6; ++ft) st.bias[ft] = unpack4(lds_b64(c.brd + bo + ft * 32));
      st.bias[6] = unpack4(lds_b64(c.brd_s + bo));
    }
    SB();
    kstep<true>(c, st, wsrd, xsrd, bsrd, l);
#pragma unroll 1
    for (int s = 1; s < KS; ++s) kstep<false>(c, st, wsrd, xsrd, bsrd, l);
    if (l == 0) {
#pragma unroll
      for (int t = 0; t < 26; ++t) {
        st.x0[t] = unpack4(lds_b64(tile_addr(c, t)));
        st.x[t] = st.x0[t];
      }
    }
#pragma unroll
    for (int t = 0; t < 26; ++t) {
#pragma unroll
      for (int i = 0; i < 4; ++i) st.x[t][i] = __builtin_fmaf(st.x0[t][i], st.acc[t][i], st.x[t][i]);
      u32x2 p = {pack2(st.x[t][0], st.x[t][1]), pack2(st.x[t][2], st.x[t][3])};
      lds_w64(tile_addr(c, t), p);
    }
    MEMFENCE();
    __builtin_amdgcn_s_waitcnt(waitimm(63, 0));
    __builtin_amdgcn_s_barrier();
    MEMFENCE();
    SB();
  }

  {
    const __amdgpu_buffer_rsrc_t osrd =
        __builtin_amdgcn_make_buffer_rsrc((void*)(outg + (size_t)row0 * DF), 0, BM * ROWB, 0x00020000);
    const unsigned rd = c.m0x + lane * 16;
    u32x4 v[KS];
#pragma unroll
    for (int s = 0; s < KS; ++s) v[s] = lds_u128(rd + s * 4096);
#pragma unroll
    for (int s = 0; s < KS; ++s) __builtin_amdgcn_raw_buffer_store_b128(v[s], osrd, c.xvoff + s * 64, 0, 0);
  }
  asm volatile("s_waitcnt vmcnt(0)" ::: "memory");
}

extern "C" void kernel_launch(void* const* d_in, const int* in_sizes, int n_in, void* d_out, int out_size,
                              void* d_ws, size_t ws_size, hipStream_t stream) {
  const __hip_bfloat16* x0 = (const __hip_bfloat16*)d_in[0];
  const __hip_bfloat16* ws = (const __hip_bfloat16*)d_in[1];
  const __hip_bfloat16* bs = (const __hip_bfloat16*)d_in[2];
  __hip_bfloat16* out = (__hip_bfloat16*)d_out;
  const int B = in_sizes[0] / DF;
  dcn_fused_kernel<<<B / BM, 256, 0, stream>>>(x0, ws, bs, out);
}
```

```cpp
#include <hip/hip_runtime.h>
#include <hip/hip_bf16.h>
#include <stdint.h>

#define LAS __attribute__((address_space(3)))

__global__ __launch_bounds__(256, 1) void dcn_fused_kernel(const __hip_bfloat16* __restrict__ x0g,
                                                            const __hip_bfloat16* __restrict__ wg,
                                                            const __hip_bfloat16* __restrict__ bg,
                                                            __hip_bfloat16* __restrict__ outg) {
  __shared__ __attribute__((aligned(4096))) char smem[160832];
  const unsigned lds0 = (unsigned)(size_t)(LAS char*)smem;
  const unsigned tid = threadIdx.x;
  const unsigned bid = blockIdx.x;
  asm volatile(
      "v_lshrrev_b32 v208, 6, %[tid]\n"
      "v_and_b32 v224, 63, %[tid]\n"
      "v_readfirstlane_b32 s34, v208\n"
      "s_mov_b64 s[8:9], %[wp]\n"
      "s_mov_b32 s10, 2076672\n"
      "s_mov_b32 s11, 0x20000\n"
      "s_mul_i32 s36, %[bid], 53248\n"
      "s_mov_b64 s[12:13], %[xp]\n"
      "s_add_u32 s12, s12, s36\n"
      "s_addc_u32 s13, s13, 0\n"
      "s_mov_b32 s14, 53248\n"
      "s_mov_b32 s15, 0x20000\n"
      "s_lshr_b32 s36, %[bid], 3\n"
      "s_and_b32 s36, s36, 31\n"
      "s_lshl_b32 s36, s36, 2\n"
      "s_add_u32 s36, s36, s34\n"
      "s_mul_i32 s46, s36, 2816\n"
      "v_lshlrev_b32 v209, 7, v224\n"
      "v_mov_b32 v210, 0x7f000000\n"
      "v_cmp_gt_u32 vcc, 22, v224\n"
      "s_nop 1\n"
      "v_cndmask_b32 v209, v210, v209, vcc\n"
      "v_accvgpr_write_b32 a97, v209\n"
      "buffer_load_dword a94, v209, s[8:11], s46 offen\n"
      "s_add_u32 s46, s46, 360448\n"
      "s_mul_i32 s62, s34, 6656\n"
      "v_lshlrev_b32 v254, 4, v224\n"
      "v_add_u32 v254, s62, v254\n"
      "s_mul_i32 s33, s34, 13312\n"
      "s_add_u32 s33, s33, %[lds]\n"
      "s_add_u32 m0, s33, 0\n"
      "s_add_u32 s37, s62, 0\n"
      "buffer_load_dwordx4 v254, s[12:15], s37 offen offset:0 nt lds\n"
      "buffer_load_dwordx4 v254, s[12:15], s37 offen offset:1024 nt lds\n"
      "buffer_load_dwordx4 v254, s[12:15], s37 offen offset:2048 nt lds\n"
      "buffer_load_dwordx4 v254, s[12:15], s37 offen offset:3072 nt lds\n"
      "s_add_u32 m0, s33, 4096\n"
      "s_add_u32 s37, s62, 4096\n"
      "buffer_load_dwordx4 v254, s[12:15], s37 offen offset:0 nt lds\n"
      "buffer_load_dwordx4 v254, s[12:15], s37 offen offset:1024 nt lds\n"
      "buffer_load_dwordx4 v254, s[12:15], s37 offen offset:2048 nt lds\n"
      "buffer_load_dwordx4 v254, s[12:15], s37 offen offset:3072 nt lds\n"
      "s_add_u32 m0, s33, 8192\n"
      "s_add_u32 s37, s62, 8192\n"
      "buffer_load_dwordx4 v254, s[12:15], s37 offen offset:0 nt lds\n"
      "buffer_load_dwordx4 v254, s[12:15], s37 offen offset:1024 nt lds\n"
      "buffer_load_dwordx4 v254, s[12:15], s37 offen offset:2048 nt lds\n"
      "buffer_load_dwordx4 v254, s[12:15], s37 offen offset:3072 nt lds\n"
      "s_add_u32 m0, s33, 12288\n"
      "s_add_u32 s37, s62, 12288\n"
      "buffer_load_dwordx4 v254, s[12:15], s37 offen offset:0 nt lds\n"
      "v_and_b32 v225, 15, v224\n"
      "v_lshrrev_b32 v226, 4, v224\n"
      "v_bfe_u32 v227, v224, 2, 2\n"
      "v_and_b32 v228, 3, v224\n"
      "v_bfe_u32 v229, v224, 1, 2\n"
      "v_mul_u32_u24 v208, 6672, v226\n"
      "v_and_b32 v212, 1, v226\n"
      "v_lshl_add_u32 v208, v212, 3, v208\n"
      "v_mul_u32_u24 v212, 832, v227\n"
      "v_add_u32 v208, v208, v212\n"
      "v_lshl_add_u32 v208, v228, 4, v208\n"
      "v_add_u32 v208, %[lds], v208\n"
      "v_add_u32 v245, 54016, v208\n"
      "s_mul_i32 s40, s34, 192\n"
      "s_add_u32 s36, s40, 53248\n"
      "v_add_u32 v244, s36, v208\n"
      "s_mov_b32 s56, 26688\n"
      "s_mov_b32 s57, 26688\n"
      "s_mov_b32 s58, 26688\n"
      "s_mov_b32 s59, -80064\n"
      "s_mov_b32 s60, 0\n"
      "v_xor_b32 v208, v226, v229\n"
      "v_lshlrev_b32 v208, 4, v208\n"
      "v_lshl_add_u32 v208, v225, 6, v208\n"
      "v_add_u32 v208, %[lds], v208\n"
      "v_mul_u32_u24 v213, 832, v225\n"
      "v_lshl_add_u32 v213, v226, 4, v213\n"
      "v_add_u32 v213, %[lds], v213\n"
      "s_mul_i32 s36, s34, 12288\n"
      "s_xor_b32 s38, s34, 0\n"
      "s_mul_i32 s42, s38, 13312\n"
      "s_lshl_b32 s38, s38, 10\n"
      "v_add_u32 v246, s38, v208\n"
      "v_add_u32 v250, s36, v246\n"
      "v_add_u32 v216, s42, v213\n"
      "v_add_u32 v220, s40, v216\n"
      "s_xor_b32 s38, s34, 1\n"
      "s_mul_i32 s43, s38, 13312\n"
      "s_lshl_b32 s38, s38, 10\n"
      "v_add_u32 v247, s38, v208\n"
      "v_add_u32 v251, s36, v247\n"
      "v_add_u32 v217, s43, v213\n"
      "v_add_u32 v221, s40, v217\n"
      "s_xor_b32 s38, s34, 2\n"
      "s_mul_i32 s44, s38, 13312\n"
      "s_lshl_b32 s38, s38, 10\n"
      "v_add_u32 v248, s38, v208\n"
      "v_add_u32 v252, s36, v248\n"
      "v_add_u32 v218, s44, v213\n"
      "v_add_u32 v222, s40, v218\n"
      "s_xor_b32 s38, s34, 3\n"
      "s_mul_i32 s45, s38, 13312\n"
      "s_lshl_b32 s38, s38, 10\n"
      "v_add_u32 v249, s38, v208\n"
      "v_add_u32 v253, s36, v249\n"
      "v_add_u32 v219, s45, v213\n"
      "v_add_u32 v223, s40, v219\n"
      "v_lshrrev_b32 v208, 2, v225\n"
      "v_lshlrev_b32 v208, 3, v208\n"
      "v_and_b32 v212, 3, v225\n"
      "v_add_u32 v208, v208, v212\n"
      "v_and_b32 v212, 1, v226\n"
      "v_lshl_add_u32 v208, v212, 2, v208\n"
      "v_lshlrev_b32 v208, 1, v208\n"
      "v_add_u32 v208, %[lds], v208\n"
      "v_add_u32 v208, 160000, v208\n"
      "v_add_u32 v213, 768, v208\n"
      "v_add_u32 v208, s40, v208\n"
      "v_accvgpr_write_b32 a95, v208\n"
      "v_accvgpr_write_b32 a96, v213\n"
      "s_mov_b64 s[20:21], %[bp]\n"
      "s_mov_b32 s22, 4992\n"
      "s_mov_b32 s23, 0x20000\n"
      "s_mul_i32 s32, s34, 208\n"
      "s_add_u32 s61, s32, %[lds]\n"
      "s_add_u32 s61, s61, 160000\n"
      "s_mul_i32 s36, %[bid], 53248\n"
      "s_mov_b64 s[16:17], %[op]\n"
      "s_add_u32 s16, s16, s36\n"
      "s_addc_u32 s17, s17, 0\n"
      "s_mov_b32 s18, 53248\n"
      "s_mov_b32 s19, 0x20000\n"
      "s_mov_b32 s35, 0xffff0000\n"
      "s_mov_b32 s29, 5\n"
      "v_mov_b32 v208, 0x3f80\n"
      "v_mov_b32 v212, 0\n"
      "v_accvgpr_write_b32 a88, v208\n"
      "v_accvgpr_write_b32 a89, v212\n"
      "v_accvgpr_write_b32 a91, v212\n"
      "v_accvgpr_write_b32 a93, v212\n"
      "v_accvgpr_write_b32 a81, v212\n"
      "v_accvgpr_write_b32 a83, v212\n"
      "v_accvgpr_write_b32 a85, v212\n"
      "v_accvgpr_write_b32 a87, v212\n"
      "v_cmp_eq_u32 vcc, 0, v226\n"
      "s_nop 1\n"
      "v_cndmask_b32 v213, v212, v208, vcc\n"
      "v_accvgpr_write_b32 a90, v213\n"
      "v_cmp_eq_u32 vcc, 1, v226\n"
      "s_nop 1\n"
      "v_cndmask_b32 v213, v212, v208, vcc\n"
      "v_accvgpr_write_b32 a92, v213\n"
      "v_mov_b32 v0, 1.0\n"
      "v_mov_b32 v1, 1.0\n"
      "v_mov_b32 v2, 1.0\n"
      "v_mov_b32 v3, 1.0\n"
      "v_mov_b32 v4, 1.0\n"
      "v_mov_b32 v5, 1.0\n"
      "v_mov_b32 v6, 1.0\n"
      "v_mov_b32 v7, 1.0\n"
      "v_mov_b32 v8, 1.0\n"
      "v_mov_b32 v9, 1.0\n"
      "v_mov_b32 v10, 1.0\n"
      "v_mov_b32 v11, 1.0\n"
      "v_mov_b32 v12, 1.0\n"
      "v_mov_b32 v13, 1.0\n"
      "v_mov_b32 v14, 1.0\n"
      "v_mov_b32 v15, 1.0\n"
      "v_mov_b32 v16, 1.0\n"
      "v_mov_b32 v17, 1.0\n"
      "v_mov_b32 v18, 1.0\n"
      "v_mov_b32 v19, 1.0\n"
      "v_mov_b32 v20, 1.0\n"
      "v_mov_b32 v21, 1.0\n"
      "v_mov_b32 v22, 1.0\n"
      "v_mov_b32 v23, 1.0\n"
      "v_mov_b32 v24, 1.0\n"
      "v_mov_b32 v25, 1.0\n"
      "v_mov_b32 v26, 1.0\n"
      "v_mov_b32 v27, 1.0\n"
      "v_mov_b32 v28, 1.0\n"
      "v_mov_b32 v29, 1.0\n"
      "v_mov_b32 v30, 1.0\n"
      "v_mov_b32 v31, 1.0\n"
      "v_mov_b32 v32, 1.0\n"
      "v_mov_b32 v33, 1.0\n"
      "v_mov_b32 v34, 1.0\n"
      "v_mov_b32 v35, 1.0\n"
      "v_mov_b32 v36, 1.0\n"
      "v_mov_b32 v37, 1.0\n"
      "v_mov_b32 v38, 1.0\n"
      "v_mov_b32 v39, 1.0\n"
      "v_mov_b32 v40, 1.0\n"
      "v_mov_b32 v41, 1.0\n"
      "v_mov_b32 v42, 1.0\n"
      "v_mov_b32 v43, 1.0\n"
      "v_mov_b32 v44, 1.0\n"
      "v_mov_b32 v45, 1.0\n"
      "v_mov_b32 v46, 1.0\n"
      "v_mov_b32 v47, 1.0\n"
      "v_mov_b32 v48, 1.0\n"
      "v_mov_b32 v49, 1.0\n"
      "v_mov_b32 v50, 1.0\n"
      "v_mov_b32 v51, 1.0\n"
      "v_mov_b32 v52, 1.0\n"
      "v_mov_b32 v53, 1.0\n"
      "v_mov_b32 v54, 1.0\n"
      "v_mov_b32 v55, 1.0\n"
      "v_mov_b32 v56, 1.0\n"
      "v_mov_b32 v57, 1.0\n"
      "v_mov_b32 v58, 1.0\n"
      "v_mov_b32 v59, 1.0\n"
      "v_mov_b32 v60, 1.0\n"
      "v_mov_b32 v61, 1.0\n"
      "v_mov_b32 v62, 1.0\n"
      "v_mov_b32 v63, 1.0\n"
      "v_mov_b32 v64, 1.0\n"
      "v_mov_b32 v65, 1.0\n"
      "v_mov_b32 v66, 1.0\n"
      "v_mov_b32 v67, 1.0\n"
      "v_mov_b32 v68, 1.0\n"
      "v_mov_b32 v69, 1.0\n"
      "v_mov_b32 v70, 1.0\n"
      "v_mov_b32 v71, 1.0\n"
      "v_mov_b32 v72, 1.0\n"
      "v_mov_b32 v73, 1.0\n"
      "v_mov_b32 v74, 1.0\n"
      "v_mov_b32 v75, 1.0\n"
      "v_mov_b32 v76, 1.0\n"
      "v_mov_b32 v77, 1.0\n"
      "v_mov_b32 v78, 1.0\n"
      "v_mov_b32 v79, 1.0\n"
      "v_mov_b32 v80, 1.0\n"
      "v_mov_b32 v81, 1.0\n"
      "v_mov_b32 v82, 1.0\n"
      "v_mov_b32 v83, 1.0\n"
      "v_mov_b32 v84, 1.0\n"
      "v_mov_b32 v85, 1.0\n"
      "v_mov_b32 v86, 1.0\n"
      "v_mov_b32 v87, 1.0\n"
      "v_mov_b32 v88, 1.0\n"
      "v_mov_b32 v89, 1.0\n"
      "v_mov_b32 v90, 1.0\n"
      "v_mov_b32 v91, 1.0\n"
      "v_mov_b32 v92, 1.0\n"
      "v_mov_b32 v93, 1.0\n"
      "v_mov_b32 v94, 1.0\n"
      "v_mov_b32 v95, 1.0\n"
      "v_mov_b32 v96, 1.0\n"
      "v_mov_b32 v97, 1.0\n"
      "v_mov_b32 v98, 1.0\n"
      "v_mov_b32 v99, 1.0\n"
      "v_mov_b32 v100, 1.0\n"
      "v_mov_b32 v101, 1.0\n"
      "v_mov_b32 v102, 1.0\n"
      "v_mov_b32 v103, 1.0\n"
      "s_add_u32 s52, s8, 4096\n"
      "s_addc_u32 s53, s9, 0\n"
      "s_mov_b32 s54, 2072576\n"
      "s_mov_b32 s55, 0x20000\n"
      "s_mul_i32 s39, s34, 6672\n"
      "s_and_b32 s40, s34, 1\n"
      "s_lshl_b32 s40, s40, 3\n"
      "s_add_u32 s39, s39, s40\n"
      "s_add_u32 s24, s39, %[lds]\n"
      "s_add_u32 s24, s24, 53248\n"
      "s_add_u32 s25, s24, 26688\n"
      "s_add_u32 s26, s24, 53376\n"
      "s_add_u32 s27, s24, 80064\n"
      "s_mov_b32 s30, -1\n"
      "s_mov_b32 s31, 0\n"
      "s_mov_b32 s28, 0\n"
      "s_mov_b32 m0, s24\n"
      "s_nop 0\n"
      "buffer_load_dwordx4 v254, s[8:11], s28 offen offset:0 lds\n"
      "buffer_load_dwordx4 v254, s[8:11], s28 offen offset:1024 lds\n"
      "buffer_load_dwordx4 v254, s[8:11], s28 offen offset:2048 lds\n"
      "buffer_load_dwordx4 v254, s[8:11], s28 offen offset:3072 lds\n"
      "s_add_u32 m0, m0, 4096\n"
      "s_nop 0\n"
      "buffer_load_dwordx4 v254, s[52:55], s28 offen offset:0 lds\n"
      "buffer_load_dwordx4 v254, s[52:55], s28 offen offset:1024 lds\n"
      "s_mov_b64 exec, s[30:31]\n"
      "buffer_load_dwordx4 v254, s[52:55], s28 offen offset:2048 lds\n"
      "s_mov_b64 exec, -1\n"
      "s_mov_b32 s28, 26624\n"
      "s_mov_b32 m0, s25\n"
      "s_nop 0\n"
      "buffer_load_dwordx4 v254, s[8:11], s28 offen offset:0 lds\n"
      "buffer_load_dwordx4 v254, s[8:11], s28 offen offset:1024 lds\n"
      "buffer_load_dwordx4 v254, s[8:11], s28 offen offset:2048 lds\n"
      "buffer_load_dwordx4 v254, s[8:11], s28 offen offset:3072 lds\n"
      "s_add_u32 m0, m0, 4096\n"
      "s_nop 0\n"
      "buffer_load_dwordx4 v254, s[52:55], s28 offen offset:0 lds\n"
      "buffer_load_dwordx4 v254, s[52:55], s28 offen offset:1024 lds\n"
      "s_mov_b64 exec, s[30:31]\n"
      "buffer_load_dwordx4 v254, s[52:55], s28 offen offset:2048 lds\n"
      "s_mov_b64 exec, -1\n"
      "v_subrev_u32 v208, s62, v254\n"
      "s_mov_b32 m0, s61\n"
      "s_mov_b64 exec, 0x1fff\n"
      "buffer_load_dwordx4 v208, s[20:23], s32 offen lds\n"
      "s_mov_b64 exec, -1\n"
      "s_add_u32 s32, s32, 832\n"
      "s_lshl_b32 s36, %[bid], 2\n"
      "s_add_u32 s36, s36, s34\n"
      "s_and_b32 s36, s36, 1023\n"
      "s_lshl_b32 s36, s36, 11\n"
      "v_lshlrev_b32 v214, 7, v224\n"
      "s_mov_b64 exec, 0xffff\n"
      "buffer_load_dword a94, v214, s[8:11], s36 offen\n"
      "s_mov_b64 exec, -1\n"
      "s_waitcnt vmcnt(9)\n"
      "s_barrier\n"
      "ds_read_b128 a[64:67], v216 offset:0\n"
      "ds_read_b128 a[68:71], v217 offset:0\n"
      "ds_read_b128 a[72:75], v218 offset:0\n"
      "ds_read_b128 a[76:79], v219 offset:0\n"
      "ds_read_b64_tr_b16 a[0:1], v244 offset:0\n"
      "ds_read_b64_tr_b16 a[2:3], v244 offset:3328\n"
      "ds_read_b64_tr_b16 a[4:5], v244 offset:8\n"
      "ds_read_b64_tr_b16 a[6:7], v244 offset:3336\n"
      "ds_read_b64_tr_b16 a[8:9], v244 offset:64\n"
      "ds_read_b64_tr_b16 a[10:11], v244 offset:3392\n"
      "ds_read_b64_tr_b16 a[12:13], v244 offset:72\n"
      "ds_read_b64_tr_b16 a[14:15], v244 offset:3400\n"
      "ds_read_b64_tr_b16 a[16:17], v244 offset:128\n"
      "ds_read_b64_tr_b16 a[18:19], v244 offset:3456\n"
      "ds_read_b64_tr_b16 a[20:21], v244 offset:136\n"
      "ds_read_b64_tr_b16 a[22:23], v244 offset:3464\n"
      "s_waitcnt lgkmcnt(8)\n"
      "v_mfma_f32_16x16x32_bf16 v[0:3], a[0:3], a[64:67], v[0:3]\n"
      "ds_read_b64_tr_b16 a[24:25], v245 offset:0\n"
      "v_mfma_f32_16x16x32_bf16 v[8:11], a[0:3], a[68:71], v[8:11]\n"
      "ds_read_b64_tr_b16 a[26:27], v245 offset:3328\n"
      "v_mfma_f32_16x16x32_bf16 v[48:51], a[0:3], a[72:75], v[48:51]\n"
      "ds_read_b64_tr_b16 a[28:29], v245 offset:8\n"
      "v_mfma_f32_16x16x32_bf16 v[56:59], a[0:3], a[76:79], v[56:59]\n"
      "ds_read_b64_tr_b16 a[30:31], v245 offset:3336\n"
      "v_mfma_f32_16x16x32_bf16 v[4:7], a[4:7], a[64:67], v[4:7]\n"
      "v_mfma_f32_16x16x32_bf16 v[12:15], a[4:7], a[68:71], v[12:15]\n"
      "s_mov_b32 s28, 53248\n"
      "s_mov_b32 m0, s26\n"
      "s_nop 0\n"
      "buffer_load_dwordx4 v254, s[8:11], s28 offen offset:0 lds\n"
      "buffer_load_dwordx4 v254, s[8:11], s28 offen offset:1024 lds\n"
      "buffer_load_dwordx4 v254, s[8:11], s28 offen offset:2048 lds\n"
      "buffer_load_dwordx4 v254, s[8:11], s28 offen offset:3072 lds\n"
      "s_add_u32 m0, m0, 4096\n"
      "s_nop 0\n"
      "buffer_load_dwordx4 v254, s[52:55], s28 offen offset:0 lds\n"
      "buffer_load_dwordx4 v254, s[52:55], s28 offen offset:1024 lds\n"
      "s_mov_b64 exec, s[30:31]\n"
      "buffer_load_dwordx4 v254, s[52:55], s28 offen offset:2048 lds\n"
      "s_mov_b64 exec, -1\n"
      "v_mfma_f32_16x16x32_bf16 v[52:55], a[4:7], a[72:75], v[52:55]\n"
      "s_add_u32 s28, s60, 106496\n"
      "s_mov_b32 m0, s24\n"
      "v_add_u32 v244, s56, v244\n"
      "v_add_u32 v245, s56, v245\n"
      "v_mfma_f32_16x16x32_bf16 v[60:63], a[4:7], a[76:79], v[60:63]\n"
      "s_waitcnt vmcnt(9) lgkmcnt(0)\n"
      "s_barrier\n"
      "v_mfma_f32_16x16x32_bf16 v[16:19], a[8:11], a[64:67], v[16:19]\n"
      "ds_read_b64_tr_b16 a[0:1], v244 offset:0\n"
      "v_mfma_f32_16x16x32_bf16 v[24:27], a[8:11], a[68:71], v[24:27]\n"
      "ds_read_b64_tr_b16 a[2:3], v244 offset:3328\n"
      "buffer_load_dwordx4 v254, s[8:11], s28 offen offset:0 lds\n"
      "v_mfma_f32_16x16x32_bf16 v[64:67], a[8:11], a[72:75], v[64:67]\n"
      "ds_read_b64_tr_b16 a[4:5], v244 offset:8\n"
      "v_mfma_f32_16x16x32_bf16 v[72:75], a[8:11], a[76:79], v[72:75]\n"
      "ds_read_b64_tr_b16 a[6:7], v244 offset:3336\n"
      "v_mfma_f32_16x16x32_bf16 v[20:23], a[12:15], a[64:67], v[20:23]\n"
      "ds_read_b64_tr_b16 a[8:9], v244 offset:64\n"
      "v_mfma_f32_16x16x32_bf16 v[28:31], a[12:15], a[68:71], v[28:31]\n"
      "ds_read_b64_tr_b16 a[10:11], v244 offset:3392\n"
      "buffer_load_dwordx4 v254, s[8:11], s28 offen offset:1024 lds\n"
      "v_mfma_f32_16x16x32_bf16 v[68:71], a[12:15], a[72:75], v[68:71]\n"
      "ds_read_b128 a[48:51], v216 offset:64\n"
      "v_mfma_f32_16x16x32_bf16 v[76:79], a[12:15], a[76:79], v[76:79]\n"
      "ds_read_b128 a[52:55], v217 offset:64\n"
      "v_mfma_f32_16x16x32_bf16 v[32:35], a[16:19], a[64:67], v[32:35]\n"
      "ds_read_b64_tr_b16 a[12:13], v244 offset:72\n"
      "buffer_load_dwordx4 v254, s[8:11], s28 offen offset:2048 lds\n"
      "v_mfma_f32_16x16x32_bf16 v[40:43], a[16:19], a[68:71], v[40:43]\n"
      "ds_read_b64_tr_b16 a[14:15], v244 offset:3400\n"
      "ds_read_b128 v[108:111], v220 offset:0\n"
      "v_mfma_f32_16x16x32_bf16 v[80:83], a[16:19], a[72:75], v[80:83]\n"
      "ds_read_b128 a[56:59], v218 offset:64\n"
      "v_mfma_f32_16x16x32_bf16 v[88:91], a[16:19], a[76:79], v[88:91]\n"
      "ds_read_b128 a[60:63], v219 offset:64\n"
      "buffer_load_dwordx4 v254, s[8:11], s28 offen offset:3072 lds\n"
      "v_mfma_f32_16x16x32_bf16 v[36:39], a[20:23], a[64:67], v[36:39]\n"
      "ds_read_b64_tr_b16 a[16:17], v244 offset:128\n"
      "v_mfma_f32_16x16x32_bf16 v[44:47], a[20:23], a[68:71], v[44:47]\n"
      "ds_read_b64_tr_b16 a[18:19], v244 offset:3456\n"
      "v_mfma_f32_16x16x32_bf16 v[84:87], a[20:23], a[72:75], v[84:87]\n"
      "s_add_u32 m0, m0, 4096\n"
      "v_mfma_f32_16x16x32_bf16 v[92:95], a[20:23], a[76:79], v[92:95]\n"
      "buffer_load_dwordx4 v254, s[52:55], s28 offen offset:0 lds\n"
      "v_mfma_f32_16x16x32_bf16 v[96:99], a[24:27], a[64:67], v[96:99]\n"
      "ds_read_b64_tr_b16 a[20:21], v244 offset:136\n"
      "ds_read_b128 v[116:119], v221 offset:0\n"
      "v_mfma_f32_16x16x32_bf16 v[100:103], a[28:31], a[64:67], v[100:103]\n"
      "ds_read_b64_tr_b16 a[22:23], v244 offset:3464\n"
      "s_waitcnt lgkmcnt(5)\n"
      "v_mfma_f32_16x16x32_bf16 v[0:3], a[0:3], a[48:51], v[0:3]\n"
      "ds_read_b64_tr_b16 a[24:25], v245 offset:0\n"
      "v_mfma_f32_16x16x32_bf16 v[8:11], a[0:3], a[52:55], v[8:11]\n"
      "ds_read_b64_tr_b16 a[26:27], v245 offset:3328\n"
      "buffer_load_dwordx4 v254, s[52:55], s28 offen offset:1024 lds\n"
      "v_mfma_f32_16x16x32_bf16 v[48:51], a[0:3], a[56:59], v[48:51]\n"
      "ds_read_b64_tr_b16 a[28:29], v245 offset:8\n"
      "v_mfma_f32_16x16x32_bf16 v[56:59], a[0:3], a[60:63], v[56:59]\n"
      "ds_read_b64_tr_b16 a[30:31], v245 offset:3336\n"
      "v_mfma_f32_16x16x32_bf16 v[4:7], a[4:7], a[48:51], v[4:7]\n"
      "s_mov_b64 exec, s[30:31]\n"
      "buffer_load_dwordx4 v254, s[52:55], s28 offen offset:2048 lds\n"
      "s_mov_b64 exec, -1\n"
      "v_mfma_f32_16x16x32_bf16 v[12:15], a[4:7], a[52:55], v[12:15]\n"
      "s_mov_b32 s28, 79872\n"
      "s_mov_b32 m0, s27\n"
      "s_nop 0\n"
      "buffer_load_dwordx4 v254, s[8:11], s28 offen offset:0 lds\n"
      "buffer_load_dwordx4 v254, s[8:11], s28 offen offset:1024 lds\n"
      "buffer_load_dwordx4 v254, s[8:11], s28 offen offset:2048 lds\n"
      "buffer_load_dwordx4 v254, s[8:11], s28 offen offset:3072 lds\n"
      "s_add_u32 m0, m0, 4096\n"
      "s_nop 0\n"
      "buffer_load_dwordx4 v254, s[52:55], s28 offen offset:0 lds\n"
      "buffer_load_dwordx4 v254, s[52:55], s28 offen offset:1024 lds\n"
      "s_mov_b64 exec, s[30:31]\n"
      "buffer_load_dwordx4 v254, s[52:55], s28 offen offset:2048 lds\n"
      "s_mov_b64 exec, -1\n"
      "v_mfma_f32_16x16x32_bf16 v[52:55], a[4:7], a[56:59], v[52:55]\n"
      "s_add_u32 s28, s60, 133120\n"
      "s_mov_b32 m0, s25\n"
      "v_add_u32 v244, s57, v244\n"
      "v_add_u32 v245, s57, v245\n"
      "v_mfma_f32_16x16x32_bf16 v[60:63], a[4:7], a[60:63], v[60:63]\n"
      "s_waitcnt vmcnt(14) lgkmcnt(0)\n"
      "s_barrier\n"
      "v_mfma_f32_16x16x32_bf16 v[16:19], a[8:11], a[48:51], v[16:19]\n"
      "ds_read_b64_tr_b16 a[0:1], v244 offset:0\n"
      "v_accvgpr_read_b32 v208, a95\n"
      "v_mfma_f32_16x16x32_bf16 v[24:27], a[8:11], a[52:55], v[24:27]\n"
      "ds_read_b64_tr_b16 a[2:3], v244 offset:3328\n"
      "buffer_load_dwordx4 v254, s[8:11], s28 offen offset:0 lds\n"
      "v_mfma_f32_16x16x32_bf16 v[64:67], a[8:11], a[56:59], v[64:67]\n"
      "ds_read_b64_tr_b16 a[4:5], v244 offset:8\n"
      "v_accvgpr_read_b32 v209, a96\n"
      "v_mfma_f32_16x16x32_bf16 v[72:75], a[8:11], a[60:63], v[72:75]\n"
      "ds_read_b64_tr_b16 a[6:7], v244 offset:3336\n"
      "ds_read_u16 a80, v208 offset:0\n"
      "v_mfma_f32_16x16x32_bf16 v[20:23], a[12:15], a[48:51], v[20:23]\n"
      "ds_read_b64_tr_b16 a[8:9], v244 offset:64\n"
      "ds_read_u16 a82, v208 offset:64\n"
      "v_mfma_f32_16x16x32_bf16 v[28:31], a[12:15], a[52:55], v[28:31]\n"
      "ds_read_b64_tr_b16 a[10:11], v244 offset:3392\n"
      "buffer_load_dwordx4 v254, s[8:11], s28 offen offset:1024 lds\n"
      "v_mfma_f32_16x16x32_bf16 v[68:71], a[12:15], a[56:59], v[68:71]\n"
      "ds_read_b128 a[32:35], v216 offset:128\n"
      "ds_read_u16 a84, v208 offset:128\n"
      "v_mfma_f32_16x16x32_bf16 v[76:79], a[12:15], a[60:63], v[76:79]\n"
      "ds_read_b128 a[36:39], v217 offset:128\n"
      "ds_read_u16 a86, v209 offset:0\n"
      "v_mfma_f32_16x16x32_bf16 v[32:35], a[16:19], a[48:51], v[32:35]\n"
      "ds_read_b64_tr_b16 a[12:13], v244 offset:72\n"
      "buffer_load_dwordx4 v254, s[8:11], s28 offen offset:2048 lds\n"
      "v_mfma_f32_16x16x32_bf16 v[40:43], a[16:19], a[52:55], v[40:43]\n"
      "ds_read_b64_tr_b16 a[14:15], v244 offset:3400\n"
      "ds_read_b128 v[156:159], v222 offset:0\n"
      "v_mfma_f32_16x16x32_bf16 v[80:83], a[16:19], a[56:59], v[80:83]\n"
      "ds_read_b128 a[40:43], v218 offset:128\n"
      "v_lshlrev_b32 v104, 16, v108\n"
      "v_mfma_f32_16x16x32_bf16 v[88:91], a[16:19], a[60:63], v[88:91]\n"
      "ds_read_b128 a[44:47], v219 offset:128\n"
      "buffer_load_dwordx4 v254, s[8:11], s28 offen offset:3072 lds\n"
      "v_mfma_f32_16x16x32_bf16 v[36:39], a[20:23], a[48:51], v[36:39]\n"
      "ds_read_b64_tr_b16 a[16:17], v244 offset:128\n"
      "v_and_b32 v105, s35, v108\n"
      "v_mfma_f32_16x16x32_bf16 v[44:47], a[20:23], a[52:55], v[44:47]\n"
      "ds_read_b64_tr_b16 a[18:19], v244 offset:3456\n"
      "v_lshlrev_b32 v106, 16, v109\n"
      "v_mfma_f32_16x16x32_bf16 v[84:87], a[20:23], a[56:59], v[84:87]\n"
      "s_add_u32 m0, m0, 4096\n"
      "v_and_b32 v107, s35, v109\n"
      "v_lshlrev_b32 v108, 16, v110\n"
      "v_mfma_f32_16x16x32_bf16 v[92:95], a[20:23], a[60:63], v[92:95]\n"
      "buffer_load_dwordx4 v254, s[52:55], s28 offen offset:0 lds\n"
      "v_and_b32 v109, s35, v110\n"
      "v_mfma_f32_16x16x32_bf16 v[96:99], a[24:27], a[48:51], v[96:99]\n"
      "ds_read_b64_tr_b16 a[20:21], v244 offset:136\n"
      "v_lshlrev_b32 v110, 16, v111\n"
      "v_mfma_f32_16x16x32_bf16 v[100:103], a[28:31], a[48:51], v[100:103]\n"
      "ds_read_b64_tr_b16 a[22:23], v244 offset:3464\n"
      "s_waitcnt lgkmcnt(4)\n"
      "v_and_b32 v111, s35, v111\n"
      "v_lshlrev_b32 v112, 16, v116\n"
      "v_and_b32 v113, s35, v116\n"
      "v_lshlrev_b32 v114, 16, v117\n"
      "v_and_b32 v115, s35, v117\n"
      "v_lshlrev_b32 v116, 16, v118\n"
      "v_and_b32 v117, s35, v118\n"
      "v_lshlrev_b32 v118, 16, v119\n"
      "v_and_b32 v119, s35, v119\n"
      "v_mfma_f32_16x16x32_bf16 v[0:3], a[0:3], a[32:35], v[0:3]\n"
      "ds_read_b64_tr_b16 a[24:25], v245 offset:0\n"
      "v_mfma_f32_16x16x32_bf16 v[8:11], a[0:3], a[36:39], v[8:11]\n"
      "ds_read_b64_tr_b16 a[26:27], v245 offset:3328\n"
      "buffer_load_dwordx4 v254, s[52:55], s28 offen offset:1024 lds\n"
      "v_mfma_f32_16x16x32_bf16 v[48:51], a[0:3], a[40:43], v[48:51]\n"
      "ds_read_b64_tr_b16 a[28:29], v245 offset:8\n"
      "v_mfma_f32_16x16x32_bf16 v[56:59], a[0:3], a[44:47], v[56:59]\n"
      "ds_read_b64_tr_b16 a[30:31], v245 offset:3336\n"
      "v_mfma_f32_16x16x32_bf16 v[4:7], a[4:7], a[32:35], v[4:7]\n"
      "s_mov_b64 exec, s[30:31]\n"
      "buffer_load_dwordx4 v254, s[52:55], s28 offen offset:2048 lds\n"
      "s_mov_b64 exec, -1\n"
      "v_mfma_f32_16x16x32_bf16 v[12:15], a[4:7], a[36:39], v[12:15]\n"
      "s_add_u32 s28, s60, 159744\n"
      "s_mov_b32 m0, s26\n"
      "v_mfma_f32_16x16x32_bf16 v[52:55], a[4:7], a[40:43], v[52:55]\n"
      "v_add_u32 v244, s58, v244\n"
      "v_add_u32 v245, s58, v245\n"
      "v_mfma_f32_16x16x32_bf16 v[60:63], a[4:7], a[44:47], v[60:63]\n"
      "s_waitcnt vmcnt(7) lgkmcnt(0)\n"
      "s_barrier\n"
      "v_mfma_f32_16x16x32_bf16 v[16:19], a[8:11], a[32:35], v[16:19]\n"
      "ds_read_b64_tr_b16 a[0:1], v244 offset:0\n"
      "v_lshlrev_b32 v152, 16, v156\n"
      "v_mfma_f32_16x16x32_bf16 v[24:27], a[8:11], a[36:39], v[24:27]\n"
      "ds_read_b64_tr_b16 a[2:3], v244 offset:3328\n"
      "buffer_load_dwordx4 v254, s[8:11], s28 offen offset:0 lds\n"
      "v_mfma_f32_16x16x32_bf16 v[64:67], a[8:11], a[40:43], v[64:67]\n"
      "ds_read_b64_tr_b16 a[4:5], v244 offset:8\n"
      "v_and_b32 v153, s35, v156\n"
      "v_mfma_f32_16x16x32_bf16 v[72:75], a[8:11], a[44:47], v[72:75]\n"
      "ds_read_b64_tr_b16 a[6:7], v244 offset:3336\n"
      "v_lshlrev_b32 v154, 16, v157\n"
      "v_mfma_f32_16x16x32_bf16 v[20:23], a[12:15], a[32:35], v[20:23]\n"
      "ds_read_b64_tr_b16 a[8:9], v244 offset:64\n"
      "v_and_b32 v155, s35, v157\n"
      "v_mfma_f32_16x16x32_bf16 v[28:31], a[12:15], a[36:39], v[28:31]\n"
      "ds_read_b64_tr_b16 a[10:11], v244 offset:3392\n"
      "buffer_load_dwordx4 v254, s[8:11], s28 offen offset:1024 lds\n"
      "v_mfma_f32_16x16x32_bf16 v[68:71], a[12:15], a[40:43], v[68:71]\n"
      "ds_read_b128 a[48:51], v216 offset:192\n"
      "v_mfma_f32_16x16x4_4b_bf16 v[0:15], a[80:81], a[88:89], v[0:15]\n"
      "v_mfma_f32_16x16x32_bf16 v[76:79], a[12:15], a[44:47], v[76:79]\n"
      "ds_read_b128 a[52:55], v217 offset:192\n"
      "v_mfma_f32_16x16x4_4b_bf16 v[48:63], a[80:81], a[88:89], v[48:63]\n"
      "v_mfma_f32_16x16x32_bf16 v[32:35], a[16:19], a[32:35], v[32:35]\n"
      "ds_read_b64_tr_b16 a[12:13], v244 offset:72\n"
      "buffer_load_dwordx4 v254, s[8:11], s28 offen offset:2048 lds\n"
      "v_mfma_f32_16x16x32_bf16 v[40:43], a[16:19], a[36:39], v[40:43]\n"
      "ds_read_b64_tr_b16 a[14:15], v244 offset:3400\n"
      "ds_read_b128 v[164:167], v223 offset:0\n"
      "v_mfma_f32_16x16x32_bf16 v[80:83], a[16:19], a[40:43], v[80:83]\n"
      "ds_read_b128 a[56:59], v218 offset:192\n"
      "v_lshlrev_b32 v156, 16, v158\n"
      "v_mfma_f32_16x16x32_bf16 v[88:91], a[16:19], a[44:47], v[88:91]\n"
      "ds_read_b128 a[60:63], v219 offset:192\n"
      "buffer_load_dwordx4 v254, s[8:11], s28 offen offset:3072 lds\n"
      "v_mfma_f32_16x16x32_bf16 v[36:39], a[20:23], a[32:35], v[36:39]\n"
      "ds_read_b64_tr_b16 a[16:17], v244 offset:128\n"
      "v_accvgpr_read_b32 v210, a97\n"
      "v_mfma_f32_16x16x32_bf16 v[44:47], a[20:23], a[36:39], v[44:47]\n"
      "ds_read_b64_tr_b16 a[18:19], v244 offset:3456\n"
      "buffer_load_dword a94, v210, s[8:11], s46 offen\n"
      "v_mfma_f32_16x16x32_bf16 v[84:87], a[20:23], a[40:43], v[84:87]\n"
      "s_add_u32 m0, m0, 4096\n"
      "s_add_u32 s46, s46, 360448\n"
      "v_and_b32 v157, s35, v158\n"
      "v_lshlrev_b32 v158, 16, v159\n"
      "v_mfma_f32_16x16x32_bf16 v[92:95], a[20:23], a[44:47], v[92:95]\n"
      "buffer_load_dwordx4 v254, s[52:55], s28 offen offset:0 lds\n"
      "v_and_b32 v159, s35, v159\n"
      "v_mfma_f32_16x16x32_bf16 v[96:99], a[24:27], a[32:35], v[96:99]\n"
      "ds_read_b64_tr_b16 a[20:21], v244 offset:136\n"
      "ds_read_b128 v[124:127], v220 offset:64\n"
      "v_mfma_f32_16x16x32_bf16 v[100:103], a[28:31], a[32:35], v[100:103]\n"
      "ds_read_b64_tr_b16 a[22:23], v244 offset:3464\n"
      "s_waitcnt lgkmcnt(5)\n"
      "v_mfma_f32_16x16x32_bf16 v[0:3], a[0:3], a[48:51], v[0:3]\n"
      "ds_read_b64_tr_b16 a[24:25], v245 offset:0\n"
      "v_mfma_f32_16x16x32_bf16 v[8:11], a[0:3], a[52:55], v[8:11]\n"
      "ds_read_b64_tr_b16 a[26:27], v245 offset:3328\n"
      "buffer_load_dwordx4 v254, s[52:55], s28 offen offset:1024 lds\n"
      "v_mfma_f32_16x16x32_bf16 v[48:51], a[0:3], a[56:59], v[48:51]\n"
      "ds_read_b64_tr_b16 a[28:29], v245 offset:8\n"
      "v_mfma_f32_16x16x32_bf16 v[56:59], a[0:3], a[60:63], v[56:59]\n"
      "ds_read_b64_tr_b16 a[30:31], v245 offset:3336\n"
      "v_mfma_f32_16x16x32_bf16 v[4:7], a[4:7], a[48:51], v[4:7]\n"
      "s_mov_b64 exec, s[30:31]\n"
      "buffer_load_dwordx4 v254, s[52:55], s28 offen offset:2048 lds\n"
      "s_mov_b64 exec, -1\n"
      "v_mfma_f32_16x16x32_bf16 v[12:15], a[4:7], a[52:55], v[12:15]\n"
      "v_subrev_u32 v208, s62, v254\n"
      "s_mov_b32 m0, s61\n"
      "s_mov_b64 exec, 0x1fff\n"
      "buffer_load_dwordx4 v208, s[20:23], s32 offen lds\n"
      "s_mov_b64 exec, -1\n"
      "s_add_u32 s32, s32, 832\n"
      "v_mfma_f32_16x16x32_bf16 v[52:55], a[4:7], a[56:59], v[52:55]\n"
      "s_add_u32 s28, s60, 186368\n"
      "s_mov_b32 m0, s27\n"
      "v_add_u32 v244, s59, v244\n"
      "v_add_u32 v245, s59, v245\n"
      "v_mfma_f32_16x16x32_bf16 v[60:63], a[4:7], a[60:63], v[60:63]\n"
      "s_waitcnt vmcnt(23) lgkmcnt(0)\n"
      "s_barrier\n"
      "v_mfma_f32_16x16x32_bf16 v[16:19], a[8:11], a[48:51], v[16:19]\n"
      "ds_read_b64_tr_b16 a[0:1], v244 offset:0\n"
      "v_lshlrev_b32 v160, 16, v164\n"
      "v_mfma_f32_16x16x32_bf16 v[24:27], a[8:11], a[52:55], v[24:27]\n"
      "ds_read_b64_tr_b16 a[2:3], v244 offset:3328\n"
      "buffer_load_dwordx4 v254, s[8:11], s28 offen offset:0 lds\n"
      "v_mfma_f32_16x16x32_bf16 v[64:67], a[8:11], a[56:59], v[64:67]\n"
      "ds_read_b64_tr_b16 a[4:5], v244 offset:8\n"
      "v_and_b32 v161, s35, v164\n"
      "v_mfma_f32_16x16x32_bf16 v[72:75], a[8:11], a[60:63], v[72:75]\n"
      "ds_read_b64_tr_b16 a[6:7], v244 offset:3336\n"
      "v_lshlrev_b32 v162, 16, v165\n"
      "v_mfma_f32_16x16x32_bf16 v[20:23], a[12:15], a[48:51], v[20:23]\n"
      "ds_read_b64_tr_b16 a[8:9], v244 offset:64\n"
      "v_and_b32 v163, s35, v165\n"
      "v_mfma_f32_16x16x32_bf16 v[28:31], a[12:15], a[52:55], v[28:31]\n"
      "ds_read_b64_tr_b16 a[10:11], v244 offset:3392\n"
      "buffer_load_dwordx4 v254, s[8:11], s28 offen offset:1024 lds\n"
      "v_mfma_f32_16x16x32_bf16 v[68:71], a[12:15], a[56:59], v[68:71]\n"
      "ds_read_b128 a[32:35], v216 offset:256\n"
      "v_lshlrev_b32 v164, 16, v166\n"
      "v_mfma_f32_16x16x32_bf16 v[76:79], a[12:15], a[60:63], v[76:79]\n"
      "ds_read_b128 a[36:39], v217 offset:256\n"
      "v_and_b32 v165, s35, v166\n"
      "v_mfma_f32_16x16x32_bf16 v[32:35], a[16:19], a[48:51], v[32:35]\n"
      "ds_read_b64_tr_b16 a[12:13], v244 offset:72\n"
      "buffer_load_dwordx4 v254, s[8:11], s28 offen offset:2048 lds\n"
      "v_mfma_f32_16x16x32_bf16 v[40:43], a[16:19], a[52:55], v[40:43]\n"
      "ds_read_b64_tr_b16 a[14:15], v244 offset:3400\n"
      "ds_read_b128 v[132:135], v221 offset:64\n"
      "v_mfma_f32_16x16x32_bf16 v[80:83], a[16:19], a[56:59], v[80:83]\n"
      "ds_read_b128 a[40:43], v218 offset:256\n"
      "v_lshlrev_b32 v166, 16, v167\n"
      "v_mfma_f32_16x16x32_bf16 v[88:91], a[16:19], a[60:63], v[88:91]\n"
      "ds_read_b128 a[44:47], v219 offset:256\n"
      "buffer_load_dwordx4 v254, s[8:11], s28 offen offset:3072 lds\n"
      "v_mfma_f32_16x16x32_bf16 v[36:39], a[20:23], a[48:51], v[36:39]\n"
      "ds_read_b64_tr_b16 a[16:17], v244 offset:128\n"
      "v_accvgpr_read_b32 v210, a97\n"
      "v_mfma_f32_16x16x32_bf16 v[44:47], a[20:23], a[52:55], v[44:47]\n"
      "ds_read_b64_tr_b16 a[18:19], v244 offset:3456\n"
      "buffer_load_dword a94, v210, s[8:11], s46 offen\n"
      "v_mfma_f32_16x16x32_bf16 v[84:87], a[20:23], a[56:59], v[84:87]\n"
      "s_add_u32 m0, m0, 4096\n"
      "v_mfma_f32_16x16x4_4b_bf16 v[16:31], a[82:83], a[88:89], v[16:31]\n"
      "s_add_u32 s46, s46, 360448\n"
      "v_and_b32 v167, s35, v167\n"
      "v_mfma_f32_16x16x32_bf16 v[92:95], a[20:23], a[60:63], v[92:95]\n"
      "buffer_load_dwordx4 v254, s[52:55], s28 offen offset:0 lds\n"
      "v_mfma_f32_16x16x4_4b_bf16 v[64:79], a[82:83], a[88:89], v[64:79]\n"
      "v_mfma_f32_16x16x32_bf16 v[96:99], a[24:27], a[48:51], v[96:99]\n"
      "ds_read_b64_tr_b16 a[20:21], v244 offset:136\n"
      "v_lshlrev_b32 v120, 16, v124\n"
      "v_mfma_f32_16x16x32_bf16 v[100:103], a[28:31], a[48:51], v[100:103]\n"
      "ds_read_b64_tr_b16 a[22:23], v244 offset:3464\n"
      "s_waitcnt lgkmcnt(4)\n"
      "v_and_b32 v121, s35, v124\n"
      "v_lshlrev_b32 v122, 16, v125\n"
      "v_and_b32 v123, s35, v125\n"
      "v_lshlrev_b32 v124, 16, v126\n"
      "v_and_b32 v125, s35, v126\n"
      "v_lshlrev_b32 v126, 16, v127\n"
      "v_and_b32 v127, s35, v127\n"
      "v_mfma_f32_16x16x32_bf16 v[0:3], a[0:3], a[32:35], v[0:3]\n"
      "ds_read_b64_tr_b16 a[24:25], v245 offset:0\n"
      "v_mfma_f32_16x16x32_bf16 v[8:11], a[0:3], a[36:39], v[8:11]\n"
      "ds_read_b64_tr_b16 a[26:27], v245 offset:3328\n"
      "buffer_load_dwordx4 v254, s[52:55], s28 offen offset:1024 lds\n"
      "v_mfma_f32_16x16x32_bf16 v[48:51], a[0:3], a[40:43], v[48:51]\n"
      "ds_read_b64_tr_b16 a[28:29], v245 offset:8\n"
      "v_mfma_f32_16x16x32_bf16 v[56:59], a[0:3], a[44:47], v[56:59]\n"
      "ds_read_b64_tr_b16 a[30:31], v245 offset:3336\n"
      "v_mfma_f32_16x16x32_bf16 v[4:7], a[4:7], a[32:35], v[4:7]\n"
      "s_mov_b64 exec, s[30:31]\n"
      "buffer_load_dwordx4 v254, s[52:55], s28 offen offset:2048 lds\n"
      "s_mov_b64 exec, -1\n"
      "v_mfma_f32_16x16x32_bf16 v[12:15], a[4:7], a[36:39], v[12:15]\n"
      "s_add_u32 s28, s60, 212992\n"
      "s_mov_b32 m0, s24\n"
      "v_mfma_f32_16x16x32_bf16 v[52:55], a[4:7], a[40:43], v[52:55]\n"
      "v_add_u32 v244, s56, v244\n"
      "v_add_u32 v245, s56, v245\n"
      "v_mfma_f32_16x16x32_bf16 v[60:63], a[4:7], a[44:47], v[60:63]\n"
      "s_waitcnt vmcnt(17) lgkmcnt(0)\n"
      "s_barrier\n"
      "v_mfma_f32_16x16x32_bf16 v[16:19], a[8:11], a[32:35], v[16:19]\n"
      "ds_read_b64_tr_b16 a[0:1], v244 offset:0\n"
      "v_mfma_f32_16x16x4_4b_bf16 v[32:47], a[84:85], a[88:89], v[32:47]\n"
      "v_mfma_f32_16x16x32_bf16 v[24:27], a[8:11], a[36:39], v[24:27]\n"
      "ds_read_b64_tr_b16 a[2:3], v244 offset:3328\n"
      "buffer_load_dwordx4 v254, s[8:11], s28 offen offset:0 lds\n"
      "v_mfma_f32_16x16x4_4b_bf16 v[80:95], a[84:85], a[88:89], v[80:95]\n"
      "v_mfma_f32_16x16x32_bf16 v[64:67], a[8:11], a[40:43], v[64:67]\n"
      "ds_read_b64_tr_b16 a[4:5], v244 offset:8\n"
      "v_lshlrev_b32 v128, 16, v132\n"
      "v_mfma_f32_16x16x32_bf16 v[72:75], a[8:11], a[44:47], v[72:75]\n"
      "ds_read_b64_tr_b16 a[6:7], v244 offset:3336\n"
      "v_and_b32 v129, s35, v132\n"
      "v_mfma_f32_16x16x32_bf16 v[20:23], a[12:15], a[32:35], v[20:23]\n"
      "ds_read_b64_tr_b16 a[8:9], v244 offset:64\n"
      "v_lshlrev_b32 v130, 16, v133\n"
      "v_mfma_f32_16x16x32_bf16 v[28:31], a[12:15], a[36:39], v[28:31]\n"
      "ds_read_b64_tr_b16 a[10:11], v244 offset:3392\n"
      "buffer_load_dwordx4 v254, s[8:11], s28 offen offset:1024 lds\n"
      "v_mfma_f32_16x16x32_bf16 v[68:71], a[12:15], a[40:43], v[68:71]\n"
      "ds_read_b128 a[48:51], v216 offset:320\n"
      "v_and_b32 v131, s35, v133\n"
      "v_mfma_f32_16x16x32_bf16 v[76:79], a[12:15], a[44:47], v[76:79]\n"
      "ds_read_b128 a[52:55], v217 offset:320\n"
      "v_lshlrev_b32 v132, 16, v134\n"
      "v_mfma_f32_16x16x32_bf16 v[32:35], a[16:19], a[32:35], v[32:35]\n"
      "ds_read_b64_tr_b16 a[12:13], v244 offset:72\n"
      "buffer_load_dwordx4 v254, s[8:11], s28 offen offset:2048 lds\n"
      "v_mfma_f32_16x16x32_bf16 v[40:43], a[16:19], a[36:39], v[40:43]\n"
      "ds_read_b64_tr_b16 a[14:15], v244 offset:3400\n"
      "ds_read_b128 v[172:175], v222 offset:64\n"
      "v_mfma_f32_16x16x32_bf16 v[80:83], a[16:19], a[40:43], v[80:83]\n"
      "ds_read_b128 a[56:59], v218 offset:320\n"
      "v_and_b32 v133, s35, v134\n"
      "v_mfma_f32_16x16x32_bf16 v[88:91], a[16:19], a[44:47], v[88:91]\n"
      "ds_read_b128 a[60:63], v219 offset:320\n"
      "buffer_load_dwordx4 v254, s[8:11], s28 offen offset:3072 lds\n"
      "v_mfma_f32_16x16x32_bf16 v[36:39], a[20:23], a[32:35], v[36:39]\n"
      "ds_read_b64_tr_b16 a[16:17], v244 offset:128\n"
      "v_lshlrev_b32 v134, 16, v135\n"
      "v_mfma_f32_16x16x32_bf16 v[44:47], a[20:23], a[36:39], v[44:47]\n"
      "ds_read_b64_tr_b16 a[18:19], v244 offset:3456\n"
      "v_and_b32 v135, s35, v135\n"
      "v_mfma_f32_16x16x32_bf16 v[84:87], a[20:23], a[40:43], v[84:87]\n"
      "s_add_u32 m0, m0, 4096\n"
      "v_mfma_f32_16x16x32_bf16 v[92:95], a[20:23], a[44:47], v[92:95]\n"
      "buffer_load_dwordx4 v254, s[52:55], s28 offen offset:0 lds\n"
      "v_mfma_f32_16x16x32_bf16 v[96:99], a[24:27], a[32:35], v[96:99]\n"
      "ds_read_b64_tr_b16 a[20:21], v244 offset:136\n"
      "v_mfma_f32_16x16x32_bf16 v[100:103], a[28:31], a[32:35], v[100:103]\n"
      "ds_read_b64_tr_b16 a[22:23], v244 offset:3464\n"
      "s_waitcnt lgkmcnt(4)\n"
      "v_mfma_f32_16x16x32_bf16 v[0:3], a[0:3], a[48:51], v[0:3]\n"
      "ds_read_b64_tr_b16 a[24:25], v245 offset:0\n"
      "v_mfma_f32_16x16x32_bf16 v[8:11], a[0:3], a[52:55], v[8:11]\n"
      "ds_read_b64_tr_b16 a[26:27], v245 offset:3328\n"
      "buffer_load_dwordx4 v254, s[52:55], s28 offen offset:1024 lds\n"
      "v_mfma_f32_16x16x32_bf16 v[48:51], a[0:3], a[56:59], v[48:51]\n"
      "ds_read_b64_tr_b16 a[28:29], v245 offset:8\n"
      "v_mfma_f32_16x16x32_bf16 v[56:59], a[0:3], a[60:63], v[56:59]\n"
      "ds_read_b64_tr_b16 a[30:31], v245 offset:3336\n"
      "v_mfma_f32_16x16x32_bf16 v[4:7], a[4:7], a[48:51], v[4:7]\n"
      "s_mov_b64 exec, s[30:31]\n"
      "buffer_load_dwordx4 v254, s[52:55], s28 offen offset:2048 lds\n"
      "s_mov_b64 exec, -1\n"
      "v_mfma_f32_16x16x32_bf16 v[12:15], a[4:7], a[52:55], v[12:15]\n"
      "s_add_u32 s28, s60, 239616\n"
      "s_mov_b32 m0, s25\n"
      "v_mfma_f32_16x16x32_bf16 v[52:55], a[4:7], a[56:59], v[52:55]\n"
      "v_add_u32 v244, s57, v244\n"
      "v_add_u32 v245, s57, v245\n"
      "v_mfma_f32_16x16x32_bf16 v[60:63], a[4:7], a[60:63], v[60:63]\n"
      "s_waitcnt vmcnt(16) lgkmcnt(0)\n"
      "s_barrier\n"
      "v_mfma_f32_16x16x32_bf16 v[16:19], a[8:11], a[48:51], v[16:19]\n"
      "ds_read_b64_tr_b16 a[0:1], v244 offset:0\n"
      "v_lshlrev_b32 v168, 16, v172\n"
      "v_mfma_f32_16x16x32_bf16 v[24:27], a[8:11], a[52:55], v[24:27]\n"
      "ds_read_b64_tr_b16 a[2:3], v244 offset:3328\n"
      "buffer_load_dwordx4 v254, s[8:11], s28 offen offset:0 lds\n"
      "v_mfma_f32_16x16x32_bf16 v[64:67], a[8:11], a[56:59], v[64:67]\n"
      "ds_read_b64_tr_b16 a[4:5], v244 offset:8\n"
      "v_and_b32 v169, s35, v172\n"
      "v_mfma_f32_16x16x32_bf16 v[72:75], a[8:11], a[60:63], v[72:75]\n"
      "ds_read_b64_tr_b16 a[6:7], v244 offset:3336\n"
      "v_lshlrev_b32 v170, 16, v173\n"
      "v_mfma_f32_16x16x32_bf16 v[20:23], a[12:15], a[48:51], v[20:23]\n"
      "ds_read_b64_tr_b16 a[8:9], v244 offset:64\n"
      "v_mfma_f32_16x16x16_bf16 v[96:99], a[86:87], a[90:91], v[96:99]\n"
      "v_mfma_f32_16x16x32_bf16 v[28:31], a[12:15], a[52:55], v[28:31]\n"
      "ds_read_b64_tr_b16 a[10:11], v244 offset:3392\n"
      "buffer_load_dwordx4 v254, s[8:11], s28 offen offset:1024 lds\n"
      "v_mfma_f32_16x16x32_bf16 v[68:71], a[12:15], a[56:59], v[68:71]\n"
      "ds_read_b128 a[32:35], v216 offset:384\n"
      "v_mfma_f32_16x16x16_bf16 v[100:103], a[86:87], a[92:93], v[100:103]\n"
      "v_mfma_f32_16x16x32_bf16 v[76:79], a[12:15], a[60:63], v[76:79]\n"
      "ds_read_b128 a[36:39], v217 offset:384\n"
      "v_and_b32 v171, s35, v173\n"
      "v_mfma_f32_16x16x32_bf16 v[32:35], a[16:19], a[48:51], v[32:35]\n"
      "ds_read_b64_tr_b16 a[12:13], v244 offset:72\n"
      "buffer_load_dwordx4 v254, s[8:11], s28 offen offset:2048 lds\n"
      "v_mfma_f32_16x16x32_bf16 v[40:43], a[16:19], a[52:55], v[40:43]\n"
      "ds_read_b64_tr_b16 a[14:15], v244 offset:3400\n"
      "ds_read_b128 v[180:183], v223 offset:64\n"
      "v_mfma_f32_16x16x32_bf16 v[80:83], a[16:19], a[56:59], v[80:83]\n"
      "ds_read_b128 a[40:43], v218 offset:384\n"
      "v_lshlrev_b32 v172, 16, v174\n"
      "v_mfma_f32_16x16x32_bf16 v[88:91], a[16:19], a[60:63], v[88:91]\n"
      "ds_read_b128 a[44:47], v219 offset:384\n"
      "buffer_load_dwordx4 v254, s[8:11], s28 offen offset:3072 lds\n"
      "v_mfma_f32_16x16x32_bf16 v[36:39], a[20:23], a[48:51], v[36:39]\n"
      "ds_read_b64_tr_b16 a[16:17], v244 offset:128\n"
      "v_accvgpr_read_b32 v210, a97\n"
      "v_mfma_f32_16x16x32_bf16 v[44:47], a[20:23], a[52:55], v[44:47]\n"
      "ds_read_b64_tr_b16 a[18:19], v244 offset:3456\n"
      "buffer_load_dword a94, v210, s[8:11], s46 offen\n"
      "v_mfma_f32_16x16x32_bf16 v[84:87], a[20:23], a[56:59], v[84:87]\n"
      "s_add_u32 m0, m0, 4096\n"
      "s_add_u32 s46, s46, 360448\n"
      "v_and_b32 v173, s35, v174\n"
      "v_lshlrev_b32 v174, 16, v175\n"
      "v_mfma_f32_16x16x32_bf16 v[92:95], a[20:23], a[60:63], v[92:95]\n"
      "buffer_load_dwordx4 v254, s[52:55], s28 offen offset:0 lds\n"
      "v_and_b32 v175, s35, v175\n"
      "v_mfma_f32_16x16x32_bf16 v[96:99], a[24:27], a[48:51], v[96:99]\n"
      "ds_read_b64_tr_b16 a[20:21], v244 offset:136\n"
      "v_mfma_f32_16x16x32_bf16 v[100:103], a[28:31], a[48:51], v[100:103]\n"
      "ds_read_b64_tr_b16 a[22:23], v244 offset:3464\n"
      "s_waitcnt lgkmcnt(4)\n"
      "v_mfma_f32_16x16x32_bf16 v[0:3], a[0:3], a[32:35], v[0:3]\n"
      "ds_read_b64_tr_b16 a[24:25], v245 offset:0\n"
      "v_mfma_f32_16x16x32_bf16 v[8:11], a[0:3], a[36:39], v[8:11]\n"
      "ds_read_b64_tr_b16 a[26:27], v245 offset:3328\n"
      "buffer_load_dwordx4 v254, s[52:55], s28 offen offset:1024 lds\n"
      "v_mfma_f32_16x16x32_bf16 v[48:51], a[0:3], a[40:43], v[48:51]\n"
      "ds_read_b64_tr_b16 a[28:29], v245 offset:8\n"
      "v_mfma_f32_16x16x32_bf16 v[56:59], a[0:3], a[44:47], v[56:59]\n"
      "ds_read_b64_tr_b16 a[30:31], v245 offset:3336\n"
      "v_mfma_f32_16x16x32_bf16 v[4:7], a[4:7], a[32:35], v[4:7]\n"
      "s_mov_b64 exec, s[30:31]\n"
      "buffer_load_dwordx4 v254, s[52:55], s28 offen offset:2048 lds\n"
      "s_mov_b64 exec, -1\n"
      "v_mfma_f32_16x16x32_bf16 v[12:15], a[4:7], a[36:39], v[12:15]\n"
      "s_add_u32 s28, s60, 266240\n"
      "s_mov_b32 m0, s26\n"
      "v_mfma_f32_16x16x32_bf16 v[52:55], a[4:7], a[40:43], v[52:55]\n"
      "v_add_u32 v244, s58, v244\n"
      "v_add_u32 v245, s58, v245\n"
      "v_mfma_f32_16x16x32_bf16 v[60:63], a[4:7], a[44:47], v[60:63]\n"
      "s_waitcnt vmcnt(15) lgkmcnt(0)\n"
      "s_barrier\n"
      "v_mfma_f32_16x16x32_bf16 v[16:19], a[8:11], a[32:35], v[16:19]\n"
      "ds_read_b64_tr_b16 a[0:1], v244 offset:0\n"
      "v_lshlrev_b32 v176, 16, v180\n"
      "v_mfma_f32_16x16x32_bf16 v[24:27], a[8:11], a[36:39], v[24:27]\n"
      "ds_read_b64_tr_b16 a[2:3], v244 offset:3328\n"
      "buffer_load_dwordx4 v254, s[8:11], s28 offen offset:0 lds\n"
      "v_mfma_f32_16x16x32_bf16 v[64:67], a[8:11], a[40:43], v[64:67]\n"
      "ds_read_b64_tr_b16 a[4:5], v244 offset:8\n"
      "v_and_b32 v177, s35, v180\n"
      "v_mfma_f32_16x16x32_bf16 v[72:75], a[8:11], a[44:47], v[72:75]\n"
      "ds_read_b64_tr_b16 a[6:7], v244 offset:3336\n"
      "v_lshlrev_b32 v178, 16, v181\n"
      "v_mfma_f32_16x16x32_bf16 v[20:23], a[12:15], a[32:35], v[20:23]\n"
      "ds_read_b64_tr_b16 a[8:9], v244 offset:64\n"
      "v_and_b32 v179, s35, v181\n"
      "v_mfma_f32_16x16x32_bf16 v[28:31], a[12:15], a[36:39], v[28:31]\n"
      "ds_read_b64_tr_b16 a[10:11], v244 offset:3392\n"
      "buffer_load_dwordx4 v254, s[8:11], s28 offen offset:1024 lds\n"
      "v_mfma_f32_16x16x32_bf16 v[68:71], a[12:15], a[40:43], v[68:71]\n"
      "ds_read_b128 a[48:51], v216 offset:448\n"
      "v_lshlrev_b32 v180, 16, v182\n"
      "v_mfma_f32_16x16x32_bf16 v[76:79], a[12:15], a[44:47], v[76:79]\n"
      "ds_read_b128 a[52:55], v217 offset:448\n"
      "v_and_b32 v181, s35, v182\n"
      "v_mfma_f32_16x16x32_bf16 v[32:35], a[16:19], a[32:35], v[32:35]\n"
      "ds_read_b64_tr_b16 a[12:13], v244 offset:72\n"
      "buffer_load_dwordx4 v254, s[8:11], s28 offen offset:2048 lds\n"
      "v_mfma_f32_16x16x32_bf16 v[40:43], a[16:19], a[36:39], v[40:43]\n"
      "ds_read_b64_tr_b16 a[14:15], v244 offset:3400\n"
      "ds_read_b128 v[140:143], v220 offset:128\n"
      "v_mfma_f32_16x16x32_bf16 v[80:83], a[16:19], a[40:43], v[80:83]\n"
      "ds_read_b128 a[56:59], v218 offset:448\n"
      "v_lshlrev_b32 v182, 16, v183\n"
      "v_mfma_f32_16x16x32_bf16 v[88:91], a[16:19], a[44:47], v[88:91]\n"
      "ds_read_b128 a[60:63], v219 offset:448\n"
      "buffer_load_dwordx4 v254, s[8:11], s28 offen offset:3072 lds\n"
      "v_mfma_f32_16x16x32_bf16 v[36:39], a[20:23], a[32:35], v[36:39]\n"
      "ds_read_b64_tr_b16 a[16:17], v244 offset:128\n"
      "v_accvgpr_read_b32 v210, a97\n"
      "v_mfma_f32_16x16x32_bf16 v[44:47], a[20:23], a[36:39], v[44:47]\n"
      "ds_read_b64_tr_b16 a[18:19], v244 offset:3456\n"
      "buffer_load_dword a94, v210, s[8:11], s46 offen\n"
      "v_mfma_f32_16x16x32_bf16 v[84:87], a[20:23], a[40:43], v[84:87]\n"
      "s_add_u32 m0, m0, 4096\n"
      "s_add_u32 s46, s46, 360448\n"
      "v_and_b32 v183, s35, v183\n"
      "v_mfma_f32_16x16x32_bf16 v[92:95], a[20:23], a[44:47], v[92:95]\n"
      "buffer_load_dwordx4 v254, s[52:55], s28 offen offset:0 lds\n"
      "v_mfma_f32_16x16x32_bf16 v[96:99], a[24:27], a[32:35], v[96:99]\n"
      "ds_read_b64_tr_b16 a[20:21], v244 offset:136\n"
      "v_mfma_f32_16x16x32_bf16 v[100:103], a[28:31], a[32:35], v[100:103]\n"
      "ds_read_b64_tr_b16 a[22:23], v244 offset:3464\n"
      "s_waitcnt lgkmcnt(4)\n"
      "v_mfma_f32_16x16x32_bf16 v[0:3], a[0:3], a[48:51], v[0:3]\n"
      "ds_read_b64_tr_b16 a[24:25], v245 offset:0\n"
      "v_mfma_f32_16x16x32_bf16 v[8:11], a[0:3], a[52:55], v[8:11]\n"
      "ds_read_b64_tr_b16 a[26:27], v245 offset:3328\n"
      "buffer_load_dwordx4 v254, s[52:55], s28 offen offset:1024 lds\n"
      "v_mfma_f32_16x16x32_bf16 v[48:51], a[0:3], a[56:59], v[48:51]\n"
      "ds_read_b64_tr_b16 a[28:29], v245 offset:8\n"
      "v_mfma_f32_16x16x32_bf16 v[56:59], a[0:3], a[60:63], v[56:59]\n"
      "ds_read_b64_tr_b16 a[30:31], v245 offset:3336\n"
      "v_mfma_f32_16x16x32_bf16 v[4:7], a[4:7], a[48:51], v[4:7]\n"
      "s_mov_b64 exec, s[30:31]\n"
      "buffer_load_dwordx4 v254, s[52:55], s28 offen offset:2048 lds\n"
      "s_mov_b64 exec, -1\n"
      "v_mfma_f32_16x16x32_bf16 v[12:15], a[4:7], a[52:55], v[12:15]\n"
      "s_add_u32 s28, s60, 292864\n"
      "s_mov_b32 m0, s27\n"
      "v_mfma_f32_16x16x32_bf16 v[52:55], a[4:7], a[56:59], v[52:55]\n"
      "v_add_u32 v244, s59, v244\n"
      "v_add_u32 v245, s59, v245\n"
      "v_mfma_f32_16x16x32_bf16 v[60:63], a[4:7], a[60:63], v[60:63]\n"
      "s_waitcnt vmcnt(16) lgkmcnt(0)\n"
      "s_barrier\n"
      "v_mfma_f32_16x16x32_bf16 v[16:19], a[8:11], a[48:51], v[16:19]\n"
      "ds_read_b64_tr_b16 a[0:1], v244 offset:0\n"
      "v_lshlrev_b32 v136, 16, v140\n"
      "v_mfma_f32_16x16x32_bf16 v[24:27], a[8:11], a[52:55], v[24:27]\n"
      "ds_read_b64_tr_b16 a[2:3], v244 offset:3328\n"
      "buffer_load_dwordx4 v254, s[8:11], s28 offen offset:0 lds\n"
      "v_mfma_f32_16x16x32_bf16 v[64:67], a[8:11], a[56:59], v[64:67]\n"
      "ds_read_b64_tr_b16 a[4:5], v244 offset:8\n"
      "v_and_b32 v137, s35, v140\n"
      "v_mfma_f32_16x16x32_bf16 v[72:75], a[8:11], a[60:63], v[72:75]\n"
      "ds_read_b64_tr_b16 a[6:7], v244 offset:3336\n"
      "v_lshlrev_b32 v138, 16, v141\n"
      "v_mfma_f32_16x16x32_bf16 v[20:23], a[12:15], a[48:51], v[20:23]\n"
      "ds_read_b64_tr_b16 a[8:9], v244 offset:64\n"
      "v_and_b32 v139, s35, v141\n"
      "v_mfma_f32_16x16x32_bf16 v[28:31], a[12:15], a[52:55], v[28:31]\n"
      "ds_read_b64_tr_b16 a[10:11], v244 offset:3392\n"
      "buffer_load_dwordx4 v254, s[8:11], s28 offen offset:1024 lds\n"
      "v_mfma_f32_16x16x32_bf16 v[68:71], a[12:15], a[56:59], v[68:71]\n"
      "ds_read_b128 a[32:35], v216 offset:512\n"
      "v_lshlrev_b32 v140, 16, v142\n"
      "v_mfma_f32_16x16x32_bf16 v[76:79], a[12:15], a[60:63], v[76:79]\n"
      "ds_read_b128 a[36:39], v217 offset:512\n"
      "v_and_b32 v141, s35, v142\n"
      "v_mfma_f32_16x16x32_bf16 v[32:35], a[16:19], a[48:51], v[32:35]\n"
      "ds_read_b64_tr_b16 a[12:13], v244 offset:72\n"
      "buffer_load_dwordx4 v254, s[8:11], s28 offen offset:2048 lds\n"
      "v_mfma_f32_16x16x32_bf16 v[40:43], a[16:19], a[52:55], v[40:43]\n"
      "ds_read_b64_tr_b16 a[14:15], v244 offset:3400\n"
      "ds_read_b128 v[148:151], v221 offset:128\n"
      "v_mfma_f32_16x16x32_bf16 v[80:83], a[16:19], a[56:59], v[80:83]\n"
      "ds_read_b128 a[40:43], v218 offset:512\n"
      "v_lshlrev_b32 v142, 16, v143\n"
      "v_mfma_f32_16x16x32_bf16 v[88:91], a[16:19], a[60:63], v[88:91]\n"
      "ds_read_b128 a[44:47], v219 offset:512\n"
      "buffer_load_dwordx4 v254, s[8:11], s28 offen offset:3072 lds\n"
      "v_mfma_f32_16x16x32_bf16 v[36:39], a[20:23], a[48:51], v[36:39]\n"
      "ds_read_b64_tr_b16 a[16:17], v244 offset:128\n"
      "v_and_b32 v143, s35, v143\n"
      "v_mfma_f32_16x16x32_bf16 v[44:47], a[20:23], a[52:55], v[44:47]\n"
      "ds_read_b64_tr_b16 a[18:19], v244 offset:3456\n"
      "v_mfma_f32_16x16x32_bf16 v[84:87], a[20:23], a[56:59], v[84:87]\n"
      "s_add_u32 m0, m0, 4096\n"
      "v_mfma_f32_16x16x32_bf16 v[92:95], a[20:23], a[60:63], v[92:95]\n"
      "buffer_load_dwordx4 v254, s[52:55], s28 offen offset:0 lds\n"
      "v_mfma_f32_16x16x32_bf16 v[96:99], a[24:27], a[48:51], v[96:99]\n"
      "ds_read_b64_tr_b16 a[20:21], v244 offset:136\n"
      "v_mfma_f32_16x16x32_bf16 v[100:103], a[28:31], a[48:51], v[100:103]\n"
      "ds_read_b64_tr_b16 a[22:23], v244 offset:3464\n"
      "s_waitcnt lgkmcnt(4)\n"
      "v_mfma_f32_16x16x32_bf16 v[0:3], a[0:3], a[32:35], v[0:3]\n"
      "ds_read_b64_tr_b16 a[24:25], v245 offset:0\n"
      "v_mfma_f32_16x16x32_bf16 v[8:11], a[0:3], a[36:39], v[8:11]\n"
      "ds_read_b64_tr_b16 a[26:27], v245 offset:3328\n"
      "buffer_load_dwordx4 v254, s[52:55], s28 offen offset:1024 lds\n"
      "v_mfma_f32_16x16x32_bf16 v[48:51], a[0:3], a[40:43], v[48:51]\n"
      "ds_read_b64_tr_b16 a[28:29], v245 offset:8\n"
      "v_mfma_f32_16x16x32_bf16 v[56:59], a[0:3], a[44:47], v[56:59]\n"
      "ds_read_b64_tr_b16 a[30:31], v245 offset:3336\n"
      "v_mfma_f32_16x16x32_bf16 v[4:7], a[4:7], a[32:35], v[4:7]\n"
      "s_mov_b64 exec, s[30:31]\n"
      "buffer_load_dwordx4 v254, s[52:55], s28 offen offset:2048 lds\n"
      "s_mov_b64 exec, -1\n"
      "v_mfma_f32_16x16x32_bf16 v[12:15], a[4:7], a[36:39], v[12:15]\n"
      "s_add_u32 s28, s60, 319488\n"
      "s_mov_b32 m0, s24\n"
      "v_mfma_f32_16x16x32_bf16 v[52:55], a[4:7], a[40:43], v[52:55]\n"
      "v_add_u32 v244, s56, v244\n"
      "v_add_u32 v245, s56, v245\n"
      "v_mfma_f32_16x16x32_bf16 v[60:63], a[4:7], a[44:47], v[60:63]\n"
      "s_waitcnt vmcnt(15) lgkmcnt(0)\n"
      "s_barrier\n"
      "v_mfma_f32_16x16x32_bf16 v[16:19], a[8:11], a[32:35], v[16:19]\n"
      "ds_read_b64_tr_b16 a[0:1], v244 offset:0\n"
      "v_accvgpr_read_b32 v208, a95\n"
      "v_mfma_f32_16x16x32_bf16 v[24:27], a[8:11], a[36:39], v[24:27]\n"
      "ds_read_b64_tr_b16 a[2:3], v244 offset:3328\n"
      "buffer_load_dwordx4 v254, s[8:11], s28 offen offset:0 lds\n"
      "v_mfma_f32_16x16x32_bf16 v[64:67], a[8:11], a[40:43], v[64:67]\n"
      "ds_read_b64_tr_b16 a[4:5], v244 offset:8\n"
      "v_accvgpr_read_b32 v209, a96\n"
      "v_mfma_f32_16x16x32_bf16 v[72:75], a[8:11], a[44:47], v[72:75]\n"
      "ds_read_b64_tr_b16 a[6:7], v244 offset:3336\n"
      "ds_read_u16 a80, v208 offset:0\n"
      "v_mfma_f32_16x16x32_bf16 v[20:23], a[12:15], a[32:35], v[20:23]\n"
      "ds_read_b64_tr_b16 a[8:9], v244 offset:64\n"
      "ds_read_u16 a82, v208 offset:64\n"
      "v_mfma_f32_16x16x32_bf16 v[28:31], a[12:15], a[36:39], v[28:31]\n"
      "ds_read_b64_tr_b16 a[10:11], v244 offset:3392\n"
      "buffer_load_dwordx4 v254, s[8:11], s28 offen offset:1024 lds\n"
      "v_mfma_f32_16x16x32_bf16 v[68:71], a[12:15], a[40:43], v[68:71]\n"
      "ds_read_b128 a[48:51], v216 offset:576\n"
      "ds_read_u16 a84, v208 offset:128\n"
      "v_mfma_f32_16x16x32_bf16 v[76:79], a[12:15], a[44:47], v[76:79]\n"
      "ds_read_b128 a[52:55], v217 offset:576\n"
      "ds_read_u16 a86, v209 offset:0\n"
      "v_mfma_f32_16x16x32_bf16 v[32:35], a[16:19], a[32:35], v[32:35]\n"
      "ds_read_b64_tr_b16 a[12:13], v244 offset:72\n"
      "buffer_load_dwordx4 v254, s[8:11], s28 offen offset:2048 lds\n"
      "v_mfma_f32_16x16x32_bf16 v[40:43], a[16:19], a[36:39], v[40:43]\n"
      "ds_read_b64_tr_b16 a[14:15], v244 offset:3400\n"
      "ds_read_b128 v[188:191], v222 offset:128\n"
      "v_mfma_f32_16x16x32_bf16 v[80:83], a[16:19], a[40:43], v[80:83]\n"
      "ds_read_b128 a[56:59], v218 offset:576\n"
      "v_lshlrev_b32 v144, 16, v148\n"
      "v_mfma_f32_16x16x32_bf16 v[88:91], a[16:19], a[44:47], v[88:91]\n"
      "ds_read_b128 a[60:63], v219 offset:576\n"
      "buffer_load_dwordx4 v254, s[8:11], s28 offen offset:3072 lds\n"
      "v_mfma_f32_16x16x32_bf16 v[36:39], a[20:23], a[32:35], v[36:39]\n"
      "ds_read_b64_tr_b16 a[16:17], v244 offset:128\n"
      "v_accvgpr_read_b32 v210, a97\n"
      "v_mfma_f32_16x16x32_bf16 v[44:47], a[20:23], a[36:39], v[44:47]\n"
      "ds_read_b64_tr_b16 a[18:19], v244 offset:3456\n"
      "buffer_load_dword a94, v210, s[8:11], s46 offen\n"
      "v_mfma_f32_16x16x32_bf16 v[84:87], a[20:23], a[40:43], v[84:87]\n"
      "s_add_u32 m0, m0, 4096\n"
      "s_add_u32 s46, s46, 360448\n"
      "v_and_b32 v145, s35, v148\n"
      "v_lshlrev_b32 v146, 16, v149\n"
      "v_mfma_f32_16x16x32_bf16 v[92:95], a[20:23], a[44:47], v[92:95]\n"
      "buffer_load_dwordx4 v254, s[52:55], s28 offen offset:0 lds\n"
      "v_and_b32 v147, s35, v149\n"
      "v_mfma_f32_16x16x32_bf16 v[96:99], a[24:27], a[32:35], v[96:99]\n"
      "ds_read_b64_tr_b16 a[20:21], v244 offset:136\n"
      "v_lshlrev_b32 v148, 16, v150\n"
      "v_mfma_f32_16x16x32_bf16 v[100:103], a[28:31], a[32:35], v[100:103]\n"
      "ds_read_b64_tr_b16 a[22:23], v244 offset:3464\n"
      "s_waitcnt lgkmcnt(4)\n"
      "v_and_b32 v149, s35, v150\n"
      "v_lshlrev_b32 v150, 16, v151\n"
      "v_and_b32 v151, s35, v151\n"
      "v_mfma_f32_16x16x32_bf16 v[0:3], a[0:3], a[48:51], v[0:3]\n"
      "ds_read_b64_tr_b16 a[24:25], v245 offset:0\n"
      "v_mfma_f32_16x16x32_bf16 v[8:11], a[0:3], a[52:55], v[8:11]\n"
      "ds_read_b64_tr_b16 a[26:27], v245 offset:3328\n"
      "buffer_load_dwordx4 v254, s[52:55], s28 offen offset:1024 lds\n"
      "v_mfma_f32_16x16x32_bf16 v[48:51], a[0:3], a[56:59], v[48:51]\n"
      "ds_read_b64_tr_b16 a[28:29], v245 offset:8\n"
      "v_mfma_f32_16x16x32_bf16 v[56:59], a[0:3], a[60:63], v[56:59]\n"
      "ds_read_b64_tr_b16 a[30:31], v245 offset:3336\n"
      "v_mfma_f32_16x16x32_bf16 v[4:7], a[4:7], a[48:51], v[4:7]\n"
      "s_mov_b64 exec, s[30:31]\n"
      "buffer_load_dwordx4 v254, s[52:55], s28 offen offset:2048 lds\n"
      "s_mov_b64 exec, -1\n"
      "v_mfma_f32_16x16x32_bf16 v[12:15], a[4:7], a[52:55], v[12:15]\n"
      "s_add_u32 s28, s60, 346112\n"
      "s_mov_b32 m0, s25\n"
      "v_mfma_f32_16x16x32_bf16 v[52:55], a[4:7], a[56:59], v[52:55]\n"
      "v_add_u32 v244, s57, v244\n"
      "v_add_u32 v245, s57, v245\n"
      "v_mfma_f32_16x16x32_bf16 v[60:63], a[4:7], a[60:63], v[60:63]\n"
      "s_waitcnt vmcnt(15) lgkmcnt(0)\n"
      "s_barrier\n"
      "v_mfma_f32_16x16x32_bf16 v[16:19], a[8:11], a[48:51], v[16:19]\n"
      "ds_read_b64_tr_b16 a[0:1], v244 offset:0\n"
      "v_lshlrev_b32 v184, 16, v188\n"
      "v_mfma_f32_16x16x32_bf16 v[24:27], a[8:11], a[52:55], v[24:27]\n"
      "ds_read_b64_tr_b16 a[2:3], v244 offset:3328\n"
      "buffer_load_dwordx4 v254, s[8:11], s28 offen offset:0 lds\n"
      "v_mfma_f32_16x16x32_bf16 v[64:67], a[8:11], a[56:59], v[64:67]\n"
      "ds_read_b64_tr_b16 a[4:5], v244 offset:8\n"
      "v_and_b32 v185, s35, v188\n"
      "v_mfma_f32_16x16x32_bf16 v[72:75], a[8:11], a[60:63], v[72:75]\n"
      "ds_read_b64_tr_b16 a[6:7], v244 offset:3336\n"
      "v_lshlrev_b32 v186, 16, v189\n"
      "v_mfma_f32_16x16x32_bf16 v[20:23], a[12:15], a[48:51], v[20:23]\n"
      "ds_read_b64_tr_b16 a[8:9], v244 offset:64\n"
      "v_and_b32 v187, s35, v189\n"
      "v_mfma_f32_16x16x32_bf16 v[28:31], a[12:15], a[52:55], v[28:31]\n"
      "ds_read_b64_tr_b16 a[10:11], v244 offset:3392\n"
      "buffer_load_dwordx4 v254, s[8:11], s28 offen offset:1024 lds\n"
      "v_mfma_f32_16x16x32_bf16 v[68:71], a[12:15], a[56:59], v[68:71]\n"
      "ds_read_b128 a[32:35], v216 offset:640\n"
      "v_lshlrev_b32 v188, 16, v190\n"
      "v_mfma_f32_16x16x32_bf16 v[76:79], a[12:15], a[60:63], v[76:79]\n"
      "ds_read_b128 a[36:39], v217 offset:640\n"
      "v_and_b32 v189, s35, v190\n"
      "v_mfma_f32_16x16x32_bf16 v[32:35], a[16:19], a[48:51], v[32:35]\n"
      "ds_read_b64_tr_b16 a[12:13], v244 offset:72\n"
      "buffer_load_dwordx4 v254, s[8:11], s28 offen offset:2048 lds\n"
      "v_mfma_f32_16x16x32_bf16 v[40:43], a[16:19], a[52:55], v[40:43]\n"
      "ds_read_b64_tr_b16 a[14:15], v244 offset:3400\n"
      "ds_read_b128 v[196:199], v223 offset:128\n"
      "v_mfma_f32_16x16x32_bf16 v[80:83], a[16:19], a[56:59], v[80:83]\n"
      "ds_read_b128 a[40:43], v218 offset:640\n"
      "v_lshlrev_b32 v190, 16, v191\n"
      "v_mfma_f32_16x16x32_bf16 v[88:91], a[16:19], a[60:63], v[88:91]\n"
      "ds_read_b128 a[44:47], v219 offset:640\n"
      "buffer_load_dwordx4 v254, s[8:11], s28 offen offset:3072 lds\n"
      "v_mfma_f32_16x16x32_bf16 v[36:39], a[20:23], a[48:51], v[36:39]\n"
      "ds_read_b64_tr_b16 a[16:17], v244 offset:128\n"
      "v_and_b32 v191, s35, v191\n"
      "v_mfma_f32_16x16x32_bf16 v[44:47], a[20:23], a[52:55], v[44:47]\n"
      "ds_read_b64_tr_b16 a[18:19], v244 offset:3456\n"
      "v_mfma_f32_16x16x32_bf16 v[84:87], a[20:23], a[56:59], v[84:87]\n"
      "s_add_u32 m0, m0, 4096\n"
      "v_mfma_f32_16x16x32_bf16 v[92:95], a[20:23], a[60:63], v[92:95]\n"
      "buffer_load_dwordx4 v254, s[52:55], s28 offen offset:0 lds\n"
      "v_mfma_f32_16x16x32_bf16 v[96:99], a[24:27], a[48:51], v[96:99]\n"
      "ds_read_b64_tr_b16 a[20:21], v244 offset:136\n"
      "v_mfma_f32_16x16x32_bf16 v[100:103], a[28:31], a[48:51], v[100:103]\n"
      "ds_read_b64_tr_b16 a[22:23], v244 offset:3464\n"
      "s_waitcnt lgkmcnt(4)\n"
      "v_mfma_f32_16x16x32_bf16 v[0:3], a[0:3], a[32:35], v[0:3]\n"
      "ds_read_b64_tr_b16 a[24:25], v245 offset:0\n"
      "v_mfma_f32_16x16x32_bf16 v[8:11], a[0:3], a[36:39], v[8:11]\n"
      "ds_read_b64_tr_b16 a[26:27], v245 offset:3328\n"
      "buffer_load_dwordx4 v254, s[52:55], s28 offen offset:1024 lds\n"
      "v_mfma_f32_16x16x32_bf16 v[48:51], a[0:3], a[40:43], v[48:51]\n"
      "ds_read_b64_tr_b16 a[28:29], v245 offset:8\n"
      "v_mfma_f32_16x16x32_bf16 v[56:59], a[0:3], a[44:47], v[56:59]\n"
      "ds_read_b64_tr_b16 a[30:31], v245 offset:3336\n"
      "v_mfma_f32_16x16x32_bf16 v[4:7], a[4:7], a[32:35], v[4:7]\n"
      "s_mov_b64 exec, s[30:31]\n"
      "buffer_load_dwordx4 v254, s[52:55], s28 offen offset:2048 lds\n"
      "s_mov_b64 exec, -1\n"
      "v_mfma_f32_16x16x32_bf16 v[12:15], a[4:7], a[36:39], v[12:15]\n"
      "s_add_u32 s28, s60, 372736\n"
      "s_mov_b32 m0, s26\n"
      "v_mfma_f32_16x16x32_bf16 v[52:55], a[4:7], a[40:43], v[52:55]\n"
      "v_add_u32 v244, s58, v244\n"
      "v_add_u32 v245, s58, v245\n"
      "v_mfma_f32_16x16x32_bf16 v[60:63], a[4:7], a[44:47], v[60:63]\n"
      "s_waitcnt vmcnt(15) lgkmcnt(0)\n"
      "s_barrier\n"
      "v_mfma_f32_16x16x32_bf16 v[16:19], a[8:11], a[32:35], v[16:19]\n"
      "ds_read_b64_tr_b16 a[0:1], v244 offset:0\n"
      "v_lshlrev_b32 v192, 16, v196\n"
      "v_mfma_f32_16x16x32_bf16 v[24:27], a[8:11], a[36:39], v[24:27]\n"
      "ds_read_b64_tr_b16 a[2:3], v244 offset:3328\n"
      "buffer_load_dwordx4 v254, s[8:11], s28 offen offset:0 lds\n"
      "v_mfma_f32_16x16x32_bf16 v[64:67], a[8:11], a[40:43], v[64:67]\n"
      "ds_read_b64_tr_b16 a[4:5], v244 offset:8\n"
      "v_and_b32 v193, s35, v196\n"
      "v_mfma_f32_16x16x32_bf16 v[72:75], a[8:11], a[44:47], v[72:75]\n"
      "ds_read_b64_tr_b16 a[6:7], v244 offset:3336\n"
      "v_lshlrev_b32 v194, 16, v197\n"
      "v_mfma_f32_16x16x32_bf16 v[20:23], a[12:15], a[32:35], v[20:23]\n"
      "ds_read_b64_tr_b16 a[8:9], v244 offset:64\n"
      "v_and_b32 v195, s35, v197\n"
      "v_mfma_f32_16x16x32_bf16 v[28:31], a[12:15], a[36:39], v[28:31]\n"
      "ds_read_b64_tr_b16 a[10:11], v244 offset:3392\n"
      "buffer_load_dwordx4 v254, s[8:11], s28 offen offset:1024 lds\n"
      "v_mfma_f32_16x16x32_bf16 v[68:71], a[12:15], a[40:43], v[68:71]\n"
      "ds_read_b128 a[48:51], v216 offset:704\n"
      "v_lshlrev_b32 v196, 16, v198\n"
      "v_mfma_f32_16x16x32_bf16 v[76:79], a[12:15], a[44:47], v[76:79]\n"
      "ds_read_b128 a[52:55], v217 offset:704\n"
      "v_and_b32 v197, s35, v198\n"
      "v_mfma_f32_16x16x32_bf16 v[32:35], a[16:19], a[32:35], v[32:35]\n"
      "ds_read_b64_tr_b16 a[12:13], v244 offset:72\n"
      "buffer_load_dwordx4 v254, s[8:11], s28 offen offset:2048 lds\n"
      "v_mfma_f32_16x16x32_bf16 v[40:43], a[16:19], a[36:39], v[40:43]\n"
      "ds_read_b64_tr_b16 a[14:15], v244 offset:3400\n"
      "ds_read_b128 v[204:207], v216 offset:768\n"
      "v_mfma_f32_16x16x32_bf16 v[80:83], a[16:19], a[40:43], v[80:83]\n"
      "ds_read_b128 a[56:59], v218 offset:704\n"
      "v_lshlrev_b32 v198, 16, v199\n"
      "v_mfma_f32_16x16x32_bf16 v[88:91], a[16:19], a[44:47], v[88:91]\n"
      "ds_read_b128 a[60:63], v219 offset:704\n"
      "buffer_load_dwordx4 v254, s[8:11], s28 offen offset:3072 lds\n"
      "v_mfma_f32_16x16x32_bf16 v[36:39], a[20:23], a[32:35], v[36:39]\n"
      "ds_read_b64_tr_b16 a[16:17], v244 offset:128\n"
      "v_and_b32 v199, s35, v199\n"
      "v_mfma_f32_16x16x32_bf16 v[44:47], a[20:23], a[36:39], v[44:47]\n"
      "ds_read_b64_tr_b16 a[18:19], v244 offset:3456\n"
      "v_mfma_f32_16x16x32_bf16 v[84:87], a[20:23], a[40:43], v[84:87]\n"
      "s_add_u32 m0, m0, 4096\n"
      "v_mfma_f32_16x16x32_bf16 v[92:95], a[20:23], a[44:47], v[92:95]\n"
      "buffer_load_dwordx4 v254, s[52:55], s28 offen offset:0 lds\n"
      "v_mfma_f32_16x16x32_bf16 v[96:99], a[24:27], a[32:35], v[96:99]\n"
      "ds_read_b64_tr_b16 a[20:21], v244 offset:136\n"
      "v_mfma_f32_16x16x32_bf16 v[100:103], a[28:31], a[32:35], v[100:103]\n"
      "ds_read_b64_tr_b16 a[22:23], v244 offset:3464\n"
      "s_waitcnt lgkmcnt(4)\n"
      "v_mfma_f32_16x16x32_bf16 v[0:3], a[0:3], a[48:51], v[0:3]\n"
      "ds_read_b64_tr_b16 a[24:25], v245 offset:0\n"
      "v_mfma_f32_16x16x32_bf16 v[8:11], a[0:3], a[52:55], v[8:11]\n"
      "ds_read_b64_tr_b16 a[26:27], v245 offset:3328\n"
      "buffer_load_dwordx4 v254, s[52:55], s28 offen offset:1024 lds\n"
      "v_mfma_f32_16x16x32_bf16 v[48:51], a[0:3], a[56:59], v[48:51]\n"
      "ds_read_b64_tr_b16 a[28:29], v245 offset:8\n"
      "v_mfma_f32_16x16x32_bf16 v[56:59], a[0:3], a[60:63], v[56:59]\n"
      "ds_read_b64_tr_b16 a[30:31], v245 offset:3336\n"
      "v_mfma_f32_16x16x32_bf16 v[4:7], a[4:7], a[48:51], v[4:7]\n"
      "s_mov_b64 exec, s[30:31]\n"
      "buffer_load_dwordx4 v254, s[52:55], s28 offen offset:2048 lds\n"
      "s_mov_b64 exec, -1\n"
      "v_mfma_f32_16x16x32_bf16 v[12:15], a[4:7], a[52:55], v[12:15]\n"
      "s_add_u32 s28, s60, 399360\n"
      "s_mov_b32 m0, s27\n"
      "v_mfma_f32_16x16x32_bf16 v[52:55], a[4:7], a[56:59], v[52:55]\n"
      "v_add_u32 v244, s59, v244\n"
      "v_add_u32 v245, s59, v245\n"
      "v_mfma_f32_16x16x32_bf16 v[60:63], a[4:7], a[60:63], v[60:63]\n"
      "s_waitcnt vmcnt(14) lgkmcnt(0)\n"
      "s_barrier\n"
      "v_mfma_f32_16x16x32_bf16 v[16:19], a[8:11], a[48:51], v[16:19]\n"
      "ds_read_b64_tr_b16 a[0:1], v244 offset:0\n"
      "v_lshlrev_b32 v200, 16, v204\n"
      "v_mfma_f32_16x16x32_bf16 v[24:27], a[8:11], a[52:55], v[24:27]\n"
      "ds_read_b64_tr_b16 a[2:3], v244 offset:3328\n"
      "buffer_load_dwordx4 v254, s[8:11], s28 offen offset:0 lds\n"
      "v_mfma_f32_16x16x32_bf16 v[64:67], a[8:11], a[56:59], v[64:67]\n"
      "ds_read_b64_tr_b16 a[4:5], v244 offset:8\n"
      "v_and_b32 v201, s35, v204\n"
      "v_mfma_f32_16x16x32_bf16 v[72:75], a[8:11], a[60:63], v[72:75]\n"
      "ds_read_b64_tr_b16 a[6:7], v244 offset:3336\n"
      "v_lshlrev_b32 v202, 16, v205\n"
      "v_mfma_f32_16x16x32_bf16 v[20:23], a[12:15], a[48:51], v[20:23]\n"
      "ds_read_b64_tr_b16 a[8:9], v244 offset:64\n"
      "v_and_b32 v203, s35, v205\n"
      "v_mfma_f32_16x16x32_bf16 v[28:31], a[12:15], a[52:55], v[28:31]\n"
      "ds_read_b64_tr_b16 a[10:11], v244 offset:3392\n"
      "buffer_load_dwordx4 v254, s[8:11], s28 offen offset:1024 lds\n"
      "v_mfma_f32_16x16x32_bf16 v[68:71], a[12:15], a[56:59], v[68:71]\n"
      "ds_read_b128 a[32:35], v216 offset:768\n"
      "v_lshlrev_b32 v204, 16, v206\n"
      "v_mfma_f32_16x16x32_bf16 v[76:79], a[12:15], a[60:63], v[76:79]\n"
      "ds_read_b128 a[36:39], v217 offset:768\n"
      "v_and_b32 v205, s35, v206\n"
      "v_mfma_f32_16x16x32_bf16 v[32:35], a[16:19], a[48:51], v[32:35]\n"
      "ds_read_b64_tr_b16 a[12:13], v244 offset:72\n"
      "buffer_load_dwordx4 v254, s[8:11], s28 offen offset:2048 lds\n"
      "v_mfma_f32_16x16x32_bf16 v[40:43], a[16:19], a[52:55], v[40:43]\n"
      "ds_read_b64_tr_b16 a[14:15], v244 offset:3400\n"
      "v_lshlrev_b32 v206, 16, v207\n"
      "v_mfma_f32_16x16x32_bf16 v[80:83], a[16:19], a[56:59], v[80:83]\n"
      "ds_read_b128 a[40:43], v218 offset:768\n"
      "v_and_b32 v207, s35, v207\n"
      "v_mfma_f32_16x16x32_bf16 v[88:91], a[16:19], a[60:63], v[88:91]\n"
      "ds_read_b128 a[44:47], v219 offset:768\n"
      "buffer_load_dwordx4 v254, s[8:11], s28 offen offset:3072 lds\n"
      "v_mfma_f32_16x16x32_bf16 v[36:39], a[20:23], a[48:51], v[36:39]\n"
      "ds_read_b64_tr_b16 a[16:17], v244 offset:128\n"
      "v_mfma_f32_16x16x32_bf16 v[44:47], a[20:23], a[52:55], v[44:47]\n"
      "ds_read_b64_tr_b16 a[18:19], v244 offset:3456\n"
      "v_mfma_f32_16x16x32_bf16 v[84:87], a[20:23], a[56:59], v[84:87]\n"
      "s_add_u32 m0, m0, 4096\n"
      "v_mfma_f32_16x16x32_bf16 v[92:95], a[20:23], a[60:63], v[92:95]\n"
      "buffer_load_dwordx4 v254, s[52:55], s28 offen offset:0 lds\n"
      "v_mfma_f32_16x16x32_bf16 v[96:99], a[24:27], a[48:51], v[96:99]\n"
      "ds_read_b64_tr_b16 a[20:21], v244 offset:136\n"
      "v_mfma_f32_16x16x32_bf16 v[100:103], a[28:31], a[48:51], v[100:103]\n"
      "ds_read_b64_tr_b16 a[22:23], v244 offset:3464\n"
      "s_waitcnt lgkmcnt(4)\n"
      "s_branch LS12_%=\n"
      "LTOP_%=:\n"
      "s_nop 3\n"
      "v_pk_mul_f32 v[208:209], v[32:33], v[136:137]\n"
      "v_pk_mul_f32 v[210:211], v[34:35], v[138:139]\n"
      "v_pk_mul_f32 v[212:213], v[36:37], v[140:141]\n"
      "v_pk_mul_f32 v[214:215], v[38:39], v[142:143]\n"
      "s_waitcnt lgkmcnt(7)\n"
      "v_mfma_f32_16x16x32_bf16 v[0:3], a[0:3], a[64:67], v[0:3]\n"
      "ds_read_b64_tr_b16 a[24:25], v245 offset:0\n"
      "v_mfma_f32_16x16x32_bf16 v[8:11], a[0:3], a[68:71], v[8:11]\n"
      "ds_read_b64_tr_b16 a[26:27], v245 offset:3328\n"
      "buffer_load_dwordx4 v254, s[52:55], s28 offen offset:1024 lds\n"
      "v_cvt_pk_bf16_f32 v220, v208, v209\n"
      "v_cvt_pk_bf16_f32 v221, v210, v211\n"
      "v_cvt_pk_bf16_f32 v222, v212, v213\n"
      "v_cvt_pk_bf16_f32 v223, v214, v215\n"
      "ds_write_b128 v250, v[220:223] offset:8192\n"
      "v_pk_mul_f32 v[208:209], v[40:41], v[144:145]\n"
      "v_pk_mul_f32 v[210:211], v[42:43], v[146:147]\n"
      "v_pk_mul_f32 v[212:213], v[44:45], v[148:149]\n"
      "v_pk_mul_f32 v[214:215], v[46:47], v[150:151]\n"
      "v_mfma_f32_16x16x4_4b_bf16 v[32:47], a[84:85], a[88:89], v[32:47]\n"
      "v_cvt_pk_bf16_f32 v224, v208, v209\n"
      "v_cvt_pk_bf16_f32 v225, v210, v211\n"
      "v_cvt_pk_bf16_f32 v226, v212, v213\n"
      "v_cvt_pk_bf16_f32 v227, v214, v215\n"
      "ds_write_b128 v251, v[224:227] offset:8192\n"
      "v_pk_mul_f32 v[208:209], v[80:81], v[184:185]\n"
      "v_pk_mul_f32 v[210:211], v[82:83], v[186:187]\n"
      "v_pk_mul_f32 v[212:213], v[84:85], v[188:189]\n"
      "v_pk_mul_f32 v[214:215], v[86:87], v[190:191]\n"
      "v_mfma_f32_16x16x32_bf16 v[48:51], a[0:3], a[72:75], v[48:51]\n"
      "ds_read_b64_tr_b16 a[28:29], v245 offset:8\n"
      "v_mfma_f32_16x16x32_bf16 v[56:59], a[0:3], a[76:79], v[56:59]\n"
      "ds_read_b64_tr_b16 a[30:31], v245 offset:3336\n"
      "v_cvt_pk_bf16_f32 v228, v208, v209\n"
      "v_cvt_pk_bf16_f32 v229, v210, v211\n"
      "v_cvt_pk_bf16_f32 v230, v212, v213\n"
      "v_cvt_pk_bf16_f32 v231, v214, v215\n"
      "ds_write_b128 v252, v[228:231] offset:8192\n"
      "v_pk_mul_f32 v[208:209], v[88:89], v[192:193]\n"
      "v_pk_mul_f32 v[210:211], v[90:91], v[194:195]\n"
      "v_pk_mul_f32 v[212:213], v[92:93], v[196:197]\n"
      "v_pk_mul_f32 v[214:215], v[94:95], v[198:199]\n"
      "v_mfma_f32_16x16x4_4b_bf16 v[80:95], a[84:85], a[88:89], v[80:95]\n"
      "v_cvt_pk_bf16_f32 v232, v208, v209\n"
      "v_cvt_pk_bf16_f32 v233, v210, v211\n"
      "v_cvt_pk_bf16_f32 v234, v212, v213\n"
      "v_cvt_pk_bf16_f32 v235, v214, v215\n"
      "ds_write_b128 v253, v[232:235] offset:8192\n"
      "v_pk_mul_f32 v[208:209], v[96:97], v[200:201]\n"
      "v_pk_mul_f32 v[210:211], v[98:99], v[202:203]\n"
      "v_pk_mul_f32 v[212:213], v[100:101], v[204:205]\n"
      "v_pk_mul_f32 v[214:215], v[102:103], v[206:207]\n"
      "v_mfma_f32_16x16x16_bf16 v[96:99], a[86:87], a[90:91], v[96:99]\n"
      "v_mfma_f32_16x16x16_bf16 v[100:103], a[86:87], a[92:93], v[100:103]\n"
      "v_cvt_pk_bf16_f32 v236, v208, v209\n"
      "v_cvt_pk_bf16_f32 v237, v210, v211\n"
      "v_cvt_pk_bf16_f32 v238, v212, v213\n"
      "v_cvt_pk_bf16_f32 v239, v214, v215\n"
      "ds_write_b128 v246, v[236:239] offset:49152\n"
      "v_mfma_f32_16x16x32_bf16 v[4:7], a[4:7], a[64:67], v[4:7]\n"
      "s_mov_b64 exec, s[30:31]\n"
      "buffer_load_dwordx4 v254, s[52:55], s28 offen offset:2048 lds\n"
      "s_mov_b64 exec, -1\n"
      "v_mfma_f32_16x16x32_bf16 v[12:15], a[4:7], a[68:71], v[12:15]\n"
      "s_add_u32 s28, s60, 106496\n"
      "s_mov_b32 m0, s24\n"
      "v_mfma_f32_16x16x32_bf16 v[52:55], a[4:7], a[72:75], v[52:55]\n"
      "v_add_u32 v244, s56, v244\n"
      "v_add_u32 v245, s56, v245\n"
      "v_mfma_f32_16x16x32_bf16 v[60:63], a[4:7], a[76:79], v[60:63]\n"
      "s_waitcnt vmcnt(14) lgkmcnt(0)\n"
      "s_barrier\n"
      "v_mfma_f32_16x16x32_bf16 v[16:19], a[8:11], a[64:67], v[16:19]\n"
      "ds_read_b64_tr_b16 a[0:1], v244 offset:0\n"
      "v_mfma_f32_16x16x32_bf16 v[24:27], a[8:11], a[68:71], v[24:27]\n"
      "ds_read_b64_tr_b16 a[2:3], v244 offset:3328\n"
      "buffer_load_dwordx4 v254, s[8:11], s28 offen offset:0 lds\n"
      "v_mfma_f32_16x16x32_bf16 v[64:67], a[8:11], a[72:75], v[64:67]\n"
      "ds_read_b64_tr_b16 a[4:5], v244 offset:8\n"
      "v_mfma_f32_16x16x32_bf16 v[72:75], a[8:11], a[76:79], v[72:75]\n"
      "ds_read_b64_tr_b16 a[6:7], v244 offset:3336\n"
      "v_mfma_f32_16x16x32_bf16 v[20:23], a[12:15], a[64:67], v[20:23]\n"
      "ds_read_b64_tr_b16 a[8:9], v244 offset:64\n"
      "v_mfma_f32_16x16x32_bf16 v[28:31], a[12:15], a[68:71], v[28:31]\n"
      "ds_read_b64_tr_b16 a[10:11], v244 offset:3392\n"
      "buffer_load_dwordx4 v254, s[8:11], s28 offen offset:1024 lds\n"
      "v_mfma_f32_16x16x32_bf16 v[68:71], a[12:15], a[72:75], v[68:71]\n"
      "ds_read_b128 a[48:51], v246 offset:4096\n"
      "v_mfma_f32_16x16x32_bf16 v[76:79], a[12:15], a[76:79], v[76:79]\n"
      "ds_read_b128 a[52:55], v247 offset:4096\n"
      "v_mfma_f32_16x16x32_bf16 v[32:35], a[16:19], a[64:67], v[32:35]\n"
      "ds_read_b64_tr_b16 a[12:13], v244 offset:72\n"
      "buffer_load_dwordx4 v254, s[8:11], s28 offen offset:2048 lds\n"
      "v_mfma_f32_16x16x32_bf16 v[40:43], a[16:19], a[68:71], v[40:43]\n"
      "ds_read_b64_tr_b16 a[14:15], v244 offset:3400\n"
      "v_mfma_f32_16x16x32_bf16 v[80:83], a[16:19], a[72:75], v[80:83]\n"
      "ds_read_b128 a[56:59], v248 offset:4096\n"
      "v_mfma_f32_16x16x32_bf16 v[88:91], a[16:19], a[76:79], v[88:91]\n"
      "ds_read_b128 a[60:63], v249 offset:4096\n"
      "buffer_load_dwordx4 v254, s[8:11], s28 offen offset:3072 lds\n"
      "v_mfma_f32_16x16x32_bf16 v[36:39], a[20:23], a[64:67], v[36:39]\n"
      "ds_read_b64_tr_b16 a[16:17], v244 offset:128\n"
      "v_mfma_f32_16x16x32_bf16 v[44:47], a[20:23], a[68:71], v[44:47]\n"
      "ds_read_b64_tr_b16 a[18:19], v244 offset:3456\n"
      "v_mfma_f32_16x16x32_bf16 v[84:87], a[20:23], a[72:75], v[84:87]\n"
      "s_add_u32 m0, m0, 4096\n"
      "v_mfma_f32_16x16x32_bf16 v[92:95], a[20:23], a[76:79], v[92:95]\n"
      "buffer_load_dwordx4 v254, s[52:55], s28 offen offset:0 lds\n"
      "v_mfma_f32_16x16x32_bf16 v[96:99], a[24:27], a[64:67], v[96:99]\n"
      "ds_read_b64_tr_b16 a[20:21], v244 offset:136\n"
      "v_mfma_f32_16x16x32_bf16 v[100:103], a[28:31], a[64:67], v[100:103]\n"
      "ds_read_b64_tr_b16 a[22:23], v244 offset:3464\n"
      "s_waitcnt lgkmcnt(4)\n"
      "v_mfma_f32_16x16x32_bf16 v[0:3], a[0:3], a[48:51], v[0:3]\n"
      "ds_read_b64_tr_b16 a[24:25], v245 offset:0\n"
      "v_mfma_f32_16x16x32_bf16 v[8:11], a[0:3], a[52:55], v[8:11]\n"
      "ds_read_b64_tr_b16 a[26:27], v245 offset:3328\n"
      "buffer_load_dwordx4 v254, s[52:55], s28 offen offset:1024 lds\n"
      "v_mfma_f32_16x16x32_bf16 v[48:51], a[0:3], a[56:59], v[48:51]\n"
      "ds_read_b64_tr_b16 a[28:29], v245 offset:8\n"
      "v_mfma_f32_16x16x32_bf16 v[56:59], a[0:3], a[60:63], v[56:59]\n"
      "ds_read_b64_tr_b16 a[30:31], v245 offset:3336\n"
      "v_mfma_f32_16x16x32_bf16 v[4:7], a[4:7], a[48:51], v[4:7]\n"
      "s_mov_b64 exec, s[30:31]\n"
      "buffer_load_dwordx4 v254, s[52:55], s28 offen offset:2048 lds\n"
      "s_mov_b64 exec, -1\n"
      "v_mfma_f32_16x16x32_bf16 v[12:15], a[4:7], a[52:55], v[12:15]\n"
      "s_add_u32 s28, s60, 133120\n"
      "s_mov_b32 m0, s25\n"
      "v_mfma_f32_16x16x32_bf16 v[52:55], a[4:7], a[56:59], v[52:55]\n"
      "v_add_u32 v244, s57, v244\n"
      "v_add_u32 v245, s57, v245\n"
      "v_mfma_f32_16x16x32_bf16 v[60:63], a[4:7], a[60:63], v[60:63]\n"
      "s_waitcnt vmcnt(14) lgkmcnt(0)\n"
      "s_barrier\n"
      "v_mfma_f32_16x16x32_bf16 v[16:19], a[8:11], a[48:51], v[16:19]\n"
      "ds_read_b64_tr_b16 a[0:1], v244 offset:0\n"
      "v_mfma_f32_16x16x32_bf16 v[24:27], a[8:11], a[52:55], v[24:27]\n"
      "ds_read_b64_tr_b16 a[2:3], v244 offset:3328\n"
      "buffer_load_dwordx4 v254, s[8:11], s28 offen offset:0 lds\n"
      "v_mfma_f32_16x16x32_bf16 v[64:67], a[8:11], a[56:59], v[64:67]\n"
      "ds_read_b64_tr_b16 a[4:5], v244 offset:8\n"
      "v_mfma_f32_16x16x32_bf16 v[72:75], a[8:11], a[60:63], v[72:75]\n"
      "ds_read_b64_tr_b16 a[6:7], v244 offset:3336\n"
      "v_mfma_f32_16x16x32_bf16 v[20:23], a[12:15], a[48:51], v[20:23]\n"
      "ds_read_b64_tr_b16 a[8:9], v244 offset:64\n"
      "v_mfma_f32_16x16x32_bf16 v[28:31], a[12:15], a[52:55], v[28:31]\n"
      "ds_read_b64_tr_b16 a[10:11], v244 offset:3392\n"
      "buffer_load_dwordx4 v254, s[8:11], s28 offen offset:1024 lds\n"
      "v_mfma_f32_16x16x32_bf16 v[68:71], a[12:15], a[56:59], v[68:71]\n"
      "ds_read_b128 a[32:35], v246 offset:8192\n"
      "v_mfma_f32_16x16x32_bf16 v[76:79], a[12:15], a[60:63], v[76:79]\n"
      "ds_read_b128 a[36:39], v247 offset:8192\n"
      "v_mfma_f32_16x16x32_bf16 v[32:35], a[16:19], a[48:51], v[32:35]\n"
      "ds_read_b64_tr_b16 a[12:13], v244 offset:72\n"
      "buffer_load_dwordx4 v254, s[8:11], s28 offen offset:2048 lds\n"
      "v_mfma_f32_16x16x32_bf16 v[40:43], a[16:19], a[52:55], v[40:43]\n"
      "ds_read_b64_tr_b16 a[14:15], v244 offset:3400\n"
      "v_mfma_f32_16x16x32_bf16 v[80:83], a[16:19], a[56:59], v[80:83]\n"
      "ds_read_b128 a[40:43], v248 offset:8192\n"
      "v_mfma_f32_16x16x32_bf16 v[88:91], a[16:19], a[60:63], v[88:91]\n"
      "ds_read_b128 a[44:47], v249 offset:8192\n"
      "buffer_load_dwordx4 v254, s[8:11], s28 offen offset:3072 lds\n"
      "v_mfma_f32_16x16x32_bf16 v[36:39], a[20:23], a[48:51], v[36:39]\n"
      "ds_read_b64_tr_b16 a[16:17], v244 offset:128\n"
      "v_mfma_f32_16x16x32_bf16 v[44:47], a[20:23], a[52:55], v[44:47]\n"
      "ds_read_b64_tr_b16 a[18:19], v244 offset:3456\n"
      "v_mfma_f32_16x16x32_bf16 v[84:87], a[20:23], a[56:59], v[84:87]\n"
      "s_add_u32 m0, m0, 4096\n"
      "v_mfma_f32_16x16x32_bf16 v[92:95], a[20:23], a[60:63], v[92:95]\n"
      "buffer_load_dwordx4 v254, s[52:55], s28 offen offset:0 lds\n"
      "v_mfma_f32_16x16x32_bf16 v[96:99], a[24:27], a[48:51], v[96:99]\n"
      "ds_read_b64_tr_b16 a[20:21], v244 offset:136\n"
      "v_mfma_f32_16x16x32_bf16 v[100:103], a[28:31], a[48:51], v[100:103]\n"
      "ds_read_b64_tr_b16 a[22:23], v244 offset:3464\n"
      "s_waitcnt lgkmcnt(4)\n"
      "v_mfma_f32_16x16x32_bf16 v[0:3], a[0:3], a[32:35], v[0:3]\n"
      "ds_read_b64_tr_b16 a[24:25], v245 offset:0\n"
      "v_mfma_f32_16x16x32_bf16 v[8:11], a[0:3], a[36:39], v[8:11]\n"
      "ds_read_b64_tr_b16 a[26:27], v245 offset:3328\n"
      "buffer_load_dwordx4 v254, s[52:55], s28 offen offset:1024 lds\n"
      "v_mfma_f32_16x16x32_bf16 v[48:51], a[0:3], a[40:43], v[48:51]\n"
      "ds_read_b64_tr_b16 a[28:29], v245 offset:8\n"
      "v_mfma_f32_16x16x32_bf16 v[56:59], a[0:3], a[44:47], v[56:59]\n"
      "ds_read_b64_tr_b16 a[30:31], v245 offset:3336\n"
      "v_mfma_f32_16x16x32_bf16 v[4:7], a[4:7], a[32:35], v[4:7]\n"
      "s_mov_b64 exec, s[30:31]\n"
      "buffer_load_dwordx4 v254, s[52:55], s28 offen offset:2048 lds\n"
      "s_mov_b64 exec, -1\n"
      "v_mfma_f32_16x16x32_bf16 v[12:15], a[4:7], a[36:39], v[12:15]\n"
      "v_subrev_u32 v208, s62, v254\n"
      "s_mov_b32 m0, s61\n"
      "s_mov_b64 exec, 0x1fff\n"
      "buffer_load_dwordx4 v208, s[20:23], s32 offen lds\n"
      "s_mov_b64 exec, -1\n"
      "s_add_u32 s32, s32, 832\n"
      "v_mfma_f32_16x16x32_bf16 v[52:55], a[4:7], a[40:43], v[52:55]\n"
      "s_add_u32 s28, s60, 159744\n"
      "s_mov_b32 m0, s26\n"
      "v_add_u32 v244, s58, v244\n"
      "v_add_u32 v245, s58, v245\n"
      "v_mfma_f32_16x16x32_bf16 v[60:63], a[4:7], a[44:47], v[60:63]\n"
      "s_waitcnt vmcnt(15) lgkmcnt(0)\n"
      "s_barrier\n"
      "v_mfma_f32_16x16x32_bf16 v[16:19], a[8:11], a[32:35], v[16:19]\n"
      "ds_read_b64_tr_b16 a[0:1], v244 offset:0\n"
      "v_mfma_f32_16x16x32_bf16 v[24:27], a[8:11], a[36:39], v[24:27]\n"
      "ds_read_b64_tr_b16 a[2:3], v244 offset:3328\n"
      "buffer_load_dwordx4 v254, s[8:11], s28 offen offset:0 lds\n"
      "v_mfma_f32_16x16x32_bf16 v[64:67], a[8:11], a[40:43], v[64:67]\n"
      "ds_read_b64_tr_b16 a[4:5], v244 offset:8\n"
      "v_mfma_f32_16x16x32_bf16 v[72:75], a[8:11], a[44:47], v[72:75]\n"
      "ds_read_b64_tr_b16 a[6:7], v244 offset:3336\n"
      "v_mfma_f32_16x16x32_bf16 v[20:23], a[12:15], a[32:35], v[20:23]\n"
      "ds_read_b64_tr_b16 a[8:9], v244 offset:64\n"
      "v_mfma_f32_16x16x32_bf16 v[28:31], a[12:15], a[36:39], v[28:31]\n"
      "ds_read_b64_tr_b16 a[10:11], v244 offset:3392\n"
      "buffer_load_dwordx4 v254, s[8:11], s28 offen offset:1024 lds\n"
      "v_mfma_f32_16x16x32_bf16 v[68:71], a[12:15], a[40:43], v[68:71]\n"
      "ds_read_b128 a[48:51], v246 offset:12288\n"
      "v_mfma_f32_16x16x32_bf16 v[76:79], a[12:15], a[44:47], v[76:79]\n"
      "ds_read_b128 a[52:55], v247 offset:12288\n"
      "v_mfma_f32_16x16x32_bf16 v[32:35], a[16:19], a[32:35], v[32:35]\n"
      "ds_read_b64_tr_b16 a[12:13], v244 offset:72\n"
      "buffer_load_dwordx4 v254, s[8:11], s28 offen offset:2048 lds\n"
      "v_mfma_f32_16x16x32_bf16 v[40:43], a[16:19], a[36:39], v[40:43]\n"
      "ds_read_b64_tr_b16 a[14:15], v244 offset:3400\n"
      "v_mfma_f32_16x16x32_bf16 v[80:83], a[16:19], a[40:43], v[80:83]\n"
      "ds_read_b128 a[56:59], v248 offset:12288\n"
      "v_mfma_f32_16x16x32_bf16 v[88:91], a[16:19], a[44:47], v[88:91]\n"
      "ds_read_b128 a[60:63], v249 offset:12288\n"
      "buffer_load_dwordx4 v254, s[8:11], s28 offen offset:3072 lds\n"
      "v_mfma_f32_16x16x32_bf16 v[36:39], a[20:23], a[32:35], v[36:39]\n"
      "ds_read_b64_tr_b16 a[16:17], v244 offset:128\n"
      "v_mfma_f32_16x16x32_bf16 v[44:47], a[20:23], a[36:39], v[44:47]\n"
      "ds_read_b64_tr_b16 a[18:19], v244 offset:3456\n"
      "v_mfma_f32_16x16x32_bf16 v[84:87], a[20:23], a[40:43], v[84:87]\n"
      "s_add_u32 m0, m0, 4096\n"
      "v_mfma_f32_16x16x32_bf16 v[92:95], a[20:23], a[44:47], v[92:95]\n"
      "buffer_load_dwordx4 v254, s[52:55], s28 offen offset:0 lds\n"
      "v_mfma_f32_16x16x32_bf16 v[96:99], a[24:27], a[32:35], v[96:99]\n"
      "ds_read_b64_tr_b16 a[20:21], v244 offset:136\n"
      "v_mfma_f32_16x16x32_bf16 v[100:103], a[28:31], a[32:35], v[100:103]\n"
      "ds_read_b64_tr_b16 a[22:23], v244 offset:3464\n"
      "s_waitcnt lgkmcnt(4)\n"
      "v_mfma_f32_16x16x32_bf16 v[0:3], a[0:3], a[48:51], v[0:3]\n"
      "ds_read_b64_tr_b16 a[24:25], v245 offset:0\n"
      "v_mfma_f32_16x16x32_bf16 v[8:11], a[0:3], a[52:55], v[8:11]\n"
      "ds_read_b64_tr_b16 a[26:27], v245 offset:3328\n"
      "buffer_load_dwordx4 v254, s[52:55], s28 offen offset:1024 lds\n"
      "v_mfma_f32_16x16x32_bf16 v[48:51], a[0:3], a[56:59], v[48:51]\n"
      "ds_read_b64_tr_b16 a[28:29], v245 offset:8\n"
      "v_mfma_f32_16x16x32_bf16 v[56:59], a[0:3], a[60:63], v[56:59]\n"
      "ds_read_b64_tr_b16 a[30:31], v245 offset:3336\n"
      "v_mfma_f32_16x16x32_bf16 v[4:7], a[4:7], a[48:51], v[4:7]\n"
      "s_mov_b64 exec, s[30:31]\n"
      "buffer_load_dwordx4 v254, s[52:55], s28 offen offset:2048 lds\n"
      "s_mov_b64 exec, -1\n"
      "v_mfma_f32_16x16x32_bf16 v[12:15], a[4:7], a[52:55], v[12:15]\n"
      "s_add_u32 s28, s60, 186368\n"
      "s_mov_b32 m0, s27\n"
      "v_mfma_f32_16x16x32_bf16 v[52:55], a[4:7], a[56:59], v[52:55]\n"
      "v_add_u32 v244, s59, v244\n"
      "v_add_u32 v245, s59, v245\n"
      "v_mfma_f32_16x16x32_bf16 v[60:63], a[4:7], a[60:63], v[60:63]\n"
      "s_waitcnt vmcnt(15) lgkmcnt(0)\n"
      "s_barrier\n"
      "v_mfma_f32_16x16x32_bf16 v[16:19], a[8:11], a[48:51], v[16:19]\n"
      "ds_read_b64_tr_b16 a[0:1], v244 offset:0\n"
      "v_mfma_f32_16x16x32_bf16 v[24:27], a[8:11], a[52:55], v[24:27]\n"
      "ds_read_b64_tr_b16 a[2:3], v244 offset:3328\n"
      "buffer_load_dwordx4 v254, s[8:11], s28 offen offset:0 lds\n"
      "v_mfma_f32_16x16x32_bf16 v[64:67], a[8:11], a[56:59], v[64:67]\n"
      "ds_read_b64_tr_b16 a[4:5], v244 offset:8\n"
      "v_mfma_f32_16x16x32_bf16 v[72:75], a[8:11], a[60:63], v[72:75]\n"
      "ds_read_b64_tr_b16 a[6:7], v244 offset:3336\n"
      "v_mfma_f32_16x16x32_bf16 v[20:23], a[12:15], a[48:51], v[20:23]\n"
      "ds_read_b64_tr_b16 a[8:9], v244 offset:64\n"
      "v_mfma_f32_16x16x32_bf16 v[28:31], a[12:15], a[52:55], v[28:31]\n"
      "ds_read_b64_tr_b16 a[10:11], v244 offset:3392\n"
      "buffer_load_dwordx4 v254, s[8:11], s28 offen offset:1024 lds\n"
      "v_mfma_f32_16x16x32_bf16 v[68:71], a[12:15], a[56:59], v[68:71]\n"
      "ds_read_b128 a[32:35], v246 offset:16384\n"
      "v_mfma_f32_16x16x32_bf16 v[76:79], a[12:15], a[60:63], v[76:79]\n"
      "ds_read_b128 a[36:39], v247 offset:16384\n"
      "v_mfma_f32_16x16x32_bf16 v[32:35], a[16:19], a[48:51], v[32:35]\n"
      "ds_read_b64_tr_b16 a[12:13], v244 offset:72\n"
      "buffer_load_dwordx4 v254, s[8:11], s28 offen offset:2048 lds\n"
      "v_mfma_f32_16x16x32_bf16 v[40:43], a[16:19], a[52:55], v[40:43]\n"
      "ds_read_b64_tr_b16 a[14:15], v244 offset:3400\n"
      "v_mfma_f32_16x16x32_bf16 v[80:83], a[16:19], a[56:59], v[80:83]\n"
      "ds_read_b128 a[40:43], v248 offset:16384\n"
      "v_mfma_f32_16x16x32_bf16 v[88:91], a[16:19], a[60:63], v[88:91]\n"
      "ds_read_b128 a[44:47], v249 offset:16384\n"
      "buffer_load_dwordx4 v254, s[8:11], s28 offen offset:3072 lds\n"
      "v_mfma_f32_16x16x32_bf16 v[36:39], a[20:23], a[48:51], v[36:39]\n"
      "ds_read_b64_tr_b16 a[16:17], v244 offset:128\n"
      "v_mfma_f32_16x16x32_bf16 v[44:47], a[20:23], a[52:55], v[44:47]\n"
      "ds_read_b64_tr_b16 a[18:19], v244 offset:3456\n"
      "v_mfma_f32_16x16x32_bf16 v[84:87], a[20:23], a[56:59], v[84:87]\n"
      "s_add_u32 m0, m0, 4096\n"
      "v_mfma_f32_16x16x32_bf16 v[92:95], a[20:23], a[60:63], v[92:95]\n"
      "buffer_load_dwordx4 v254, s[52:55], s28 offen offset:0 lds\n"
      "v_mfma_f32_16x16x32_bf16 v[96:99], a[24:27], a[48:51], v[96:99]\n"
      "ds_read_b64_tr_b16 a[20:21], v244 offset:136\n"
      "v_mfma_f32_16x16x32_bf16 v[100:103], a[28:31], a[48:51], v[100:103]\n"
      "ds_read_b64_tr_b16 a[22:23], v244 offset:3464\n"
      "s_waitcnt lgkmcnt(4)\n"
      "v_mfma_f32_16x16x32_bf16 v[0:3], a[0:3], a[32:35], v[0:3]\n"
      "ds_read_b64_tr_b16 a[24:25], v245 offset:0\n"
      "v_mfma_f32_16x16x32_bf16 v[8:11], a[0:3], a[36:39], v[8:11]\n"
      "ds_read_b64_tr_b16 a[26:27], v245 offset:3328\n"
      "buffer_load_dwordx4 v254, s[52:55], s28 offen offset:1024 lds\n"
      "v_mfma_f32_16x16x32_bf16 v[48:51], a[0:3], a[40:43], v[48:51]\n"
      "ds_read_b64_tr_b16 a[28:29], v245 offset:8\n"
      "v_mfma_f32_16x16x32_bf16 v[56:59], a[0:3], a[44:47], v[56:59]\n"
      "ds_read_b64_tr_b16 a[30:31], v245 offset:3336\n"
      "v_mfma_f32_16x16x32_bf16 v[4:7], a[4:7], a[32:35], v[4:7]\n"
      "s_mov_b64 exec, s[30:31]\n"
      "buffer_load_dwordx4 v254, s[52:55], s28 offen offset:2048 lds\n"
      "s_mov_b64 exec, -1\n"
      "v_mfma_f32_16x16x32_bf16 v[12:15], a[4:7], a[36:39], v[12:15]\n"
      "s_add_u32 s28, s60, 212992\n"
      "s_mov_b32 m0, s24\n"
      "v_mfma_f32_16x16x32_bf16 v[52:55], a[4:7], a[40:43], v[52:55]\n"
      "v_add_u32 v244, s56, v244\n"
      "v_add_u32 v245, s56, v245\n"
      "v_mfma_f32_16x16x32_bf16 v[60:63], a[4:7], a[44:47], v[60:63]\n"
      "s_waitcnt vmcnt(15) lgkmcnt(0)\n"
      "s_barrier\n"
      "v_mfma_f32_16x16x32_bf16 v[16:19], a[8:11], a[32:35], v[16:19]\n"
      "ds_read_b64_tr_b16 a[0:1], v244 offset:0\n"
      "v_mfma_f32_16x16x32_bf16 v[24:27], a[8:11], a[36:39], v[24:27]\n"
      "ds_read_b64_tr_b16 a[2:3], v244 offset:3328\n"
      "buffer_load_dwordx4 v254, s[8:11], s28 offen offset:0 lds\n"
      "v_mfma_f32_16x16x32_bf16 v[64:67], a[8:11], a[40:43], v[64:67]\n"
      "ds_read_b64_tr_b16 a[4:5], v244 offset:8\n"
      "v_mfma_f32_16x16x32_bf16 v[72:75], a[8:11], a[44:47], v[72:75]\n"
      "ds_read_b64_tr_b16 a[6:7], v244 offset:3336\n"
      "v_mfma_f32_16x16x32_bf16 v[20:23], a[12:15], a[32:35], v[20:23]\n"
      "ds_read_b64_tr_b16 a[8:9], v244 offset:64\n"
      "v_mfma_f32_16x16x32_bf16 v[28:31], a[12:15], a[36:39], v[28:31]\n"
      "ds_read_b64_tr_b16 a[10:11], v244 offset:3392\n"
      "buffer_load_dwordx4 v254, s[8:11], s28 offen offset:1024 lds\n"
      "v_mfma_f32_16x16x32_bf16 v[68:71], a[12:15], a[40:43], v[68:71]\n"
      "ds_read_b128 a[48:51], v246 offset:20480\n"
      "v_mfma_f32_16x16x32_bf16 v[76:79], a[12:15], a[44:47], v[76:79]\n"
      "ds_read_b128 a[52:55], v247 offset:20480\n"
      "v_mfma_f32_16x16x32_bf16 v[32:35], a[16:19], a[32:35], v[32:35]\n"
      "ds_read_b64_tr_b16 a[12:13], v244 offset:72\n"
      "buffer_load_dwordx4 v254, s[8:11], s28 offen offset:2048 lds\n"
      "v_mfma_f32_16x16x32_bf16 v[40:43], a[16:19], a[36:39], v[40:43]\n"
      "ds_read_b64_tr_b16 a[14:15], v244 offset:3400\n"
      "v_mfma_f32_16x16x32_bf16 v[80:83], a[16:19], a[40:43], v[80:83]\n"
      "ds_read_b128 a[56:59], v248 offset:20480\n"
      "v_mfma_f32_16x16x32_bf16 v[88:91], a[16:19], a[44:47], v[88:91]\n"
      "ds_read_b128 a[60:63], v249 offset:20480\n"
      "buffer_load_dwordx4 v254, s[8:11], s28 offen offset:3072 lds\n"
      "v_mfma_f32_16x16x32_bf16 v[36:39], a[20:23], a[32:35], v[36:39]\n"
      "ds_read_b64_tr_b16 a[16:17], v244 offset:128\n"
      "v_mfma_f32_16x16x32_bf16 v[44:47], a[20:23], a[36:39], v[44:47]\n"
      "ds_read_b64_tr_b16 a[18:19], v244 offset:3456\n"
      "v_mfma_f32_16x16x32_bf16 v[84:87], a[20:23], a[40:43], v[84:87]\n"
      "s_add_u32 m0, m0, 4096\n"
      "v_mfma_f32_16x16x32_bf16 v[92:95], a[20:23], a[44:47], v[92:95]\n"
      "buffer_load_dwordx4 v254, s[52:55], s28 offen offset:0 lds\n"
      "v_mfma_f32_16x16x32_bf16 v[96:99], a[24:27], a[32:35], v[96:99]\n"
      "ds_read_b64_tr_b16 a[20:21], v244 offset:136\n"
      "v_mfma_f32_16x16x32_bf16 v[100:103], a[28:31], a[32:35], v[100:103]\n"
      "ds_read_b64_tr_b16 a[22:23], v244 offset:3464\n"
      "s_waitcnt lgkmcnt(4)\n"
      "v_mfma_f32_16x16x32_bf16 v[0:3], a[0:3], a[48:51], v[0:3]\n"
      "ds_read_b64_tr_b16 a[24:25], v245 offset:0\n"
      "v_mfma_f32_16x16x32_bf16 v[8:11], a[0:3], a[52:55], v[8:11]\n"
      "ds_read_b64_tr_b16 a[26:27], v245 offset:3328\n"
      "buffer_load_dwordx4 v254, s[52:55], s28 offen offset:1024 lds\n"
      "v_mfma_f32_16x16x32_bf16 v[48:51], a[0:3], a[56:59], v[48:51]\n"
      "ds_read_b64_tr_b16 a[28:29], v245 offset:8\n"
      "v_mfma_f32_16x16x32_bf16 v[56:59], a[0:3], a[60:63], v[56:59]\n"
      "ds_read_b64_tr_b16 a[30:31], v245 offset:3336\n"
      "v_mfma_f32_16x16x32_bf16 v[4:7], a[4:7], a[48:51], v[4:7]\n"
      "s_mov_b64 exec, s[30:31]\n"
      "buffer_load_dwordx4 v254, s[52:55], s28 offen offset:2048 lds\n"
      "s_mov_b64 exec, -1\n"
      "v_mfma_f32_16x16x32_bf16 v[12:15], a[4:7], a[52:55], v[12:15]\n"
      "s_add_u32 s28, s60, 239616\n"
      "s_mov_b32 m0, s25\n"
      "v_mfma_f32_16x16x32_bf16 v[52:55], a[4:7], a[56:59], v[52:55]\n"
      "v_add_u32 v244, s57, v244\n"
      "v_add_u32 v245, s57, v245\n"
      "v_mfma_f32_16x16x32_bf16 v[60:63], a[4:7], a[60:63], v[60:63]\n"
      "s_waitcnt vmcnt(14) lgkmcnt(0)\n"
      "s_barrier\n"
      "v_mfma_f32_16x16x32_bf16 v[16:19], a[8:11], a[48:51], v[16:19]\n"
      "ds_read_b64_tr_b16 a[0:1], v244 offset:0\n"
      "v_mfma_f32_16x16x32_bf16 v[24:27], a[8:11], a[52:55], v[24:27]\n"
      "ds_read_b64_tr_b16 a[2:3], v244 offset:3328\n"
      "buffer_load_dwordx4 v254, s[8:11], s28 offen offset:0 lds\n"
      "v_mfma_f32_16x16x32_bf16 v[64:67], a[8:11], a[56:59], v[64:67]\n"
      "ds_read_b64_tr_b16 a[4:5], v244 offset:8\n"
      "v_mfma_f32_16x16x32_bf16 v[72:75], a[8:11], a[60:63], v[72:75]\n"
      "ds_read_b64_tr_b16 a[6:7], v244 offset:3336\n"
      "v_mfma_f32_16x16x32_bf16 v[20:23], a[12:15], a[48:51], v[20:23]\n"
      "ds_read_b64_tr_b16 a[8:9], v244 offset:64\n"
      "v_mfma_f32_16x16x32_bf16 v[28:31], a[12:15], a[52:55], v[28:31]\n"
      "ds_read_b64_tr_b16 a[10:11], v244 offset:3392\n"
      "buffer_load_dwordx4 v254, s[8:11], s28 offen offset:1024 lds\n"
      "v_mfma_f32_16x16x32_bf16 v[68:71], a[12:15], a[56:59], v[68:71]\n"
      "ds_read_b128 a[32:35], v246 offset:24576\n"
      "v_mfma_f32_16x16x32_bf16 v[76:79], a[12:15], a[60:63], v[76:79]\n"
      "ds_read_b128 a[36:39], v247 offset:24576\n"
      "v_mfma_f32_16x16x32_bf16 v[32:35], a[16:19], a[48:51], v[32:35]\n"
      "ds_read_b64_tr_b16 a[12:13], v244 offset:72\n"
      "buffer_load_dwordx4 v254, s[8:11], s28 offen offset:2048 lds\n"
      "v_mfma_f32_16x16x32_bf16 v[40:43], a[16:19], a[52:55], v[40:43]\n"
      "ds_read_b64_tr_b16 a[14:15], v244 offset:3400\n"
      "v_mfma_f32_16x16x32_bf16 v[80:83], a[16:19], a[56:59], v[80:83]\n"
      "ds_read_b128 a[40:43], v248 offset:24576\n"
      "v_mfma_f32_16x16x32_bf16 v[88:91], a[16:19], a[60:63], v[88:91]\n"
      "ds_read_b128 a[44:47], v249 offset:24576\n"
      "buffer_load_dwordx4 v254, s[8:11], s28 offen offset:3072 lds\n"
      "v_mfma_f32_16x16x32_bf16 v[36:39], a[20:23], a[48:51], v[36:39]\n"
      "ds_read_b64_tr_b16 a[16:17], v244 offset:128\n"
      "v_mfma_f32_16x16x32_bf16 v[44:47], a[20:23], a[52:55], v[44:47]\n"
      "ds_read_b64_tr_b16 a[18:19], v244 offset:3456\n"
      "v_mfma_f32_16x16x32_bf16 v[84:87], a[20:23], a[56:59], v[84:87]\n"
      "s_add_u32 m0, m0, 4096\n"
      "v_mfma_f32_16x16x32_bf16 v[92:95], a[20:23], a[60:63], v[92:95]\n"
      "buffer_load_dwordx4 v254, s[52:55], s28 offen offset:0 lds\n"
      "v_mfma_f32_16x16x32_bf16 v[96:99], a[24:27], a[48:51], v[96:99]\n"
      "ds_read_b64_tr_b16 a[20:21], v244 offset:136\n"
      "v_mfma_f32_16x16x32_bf16 v[100:103], a[28:31], a[48:51], v[100:103]\n"
      "ds_read_b64_tr_b16 a[22:23], v244 offset:3464\n"
      "s_waitcnt lgkmcnt(4)\n"
      "v_mfma_f32_16x16x32_bf16 v[0:3], a[0:3], a[32:35], v[0:3]\n"
      "ds_read_b64_tr_b16 a[24:25], v245 offset:0\n"
      "v_mfma_f32_16x16x32_bf16 v[8:11], a[0:3], a[36:39], v[8:11]\n"
      "ds_read_b64_tr_b16 a[26:27], v245 offset:3328\n"
      "buffer_load_dwordx4 v254, s[52:55], s28 offen offset:1024 lds\n"
      "v_mfma_f32_16x16x32_bf16 v[48:51], a[0:3], a[40:43], v[48:51]\n"
      "ds_read_b64_tr_b16 a[28:29], v245 offset:8\n"
      "v_mfma_f32_16x16x32_bf16 v[56:59], a[0:3], a[44:47], v[56:59]\n"
      "ds_read_b64_tr_b16 a[30:31], v245 offset:3336\n"
      "v_mfma_f32_16x16x32_bf16 v[4:7], a[4:7], a[32:35], v[4:7]\n"
      "s_mov_b64 exec, s[30:31]\n"
      "buffer_load_dwordx4 v254, s[52:55], s28 offen offset:2048 lds\n"
      "s_mov_b64 exec, -1\n"
      "v_mfma_f32_16x16x32_bf16 v[12:15], a[4:7], a[36:39], v[12:15]\n"
      "s_add_u32 s28, s60, 266240\n"
      "s_mov_b32 m0, s26\n"
      "v_mfma_f32_16x16x32_bf16 v[52:55], a[4:7], a[40:43], v[52:55]\n"
      "v_add_u32 v244, s58, v244\n"
      "v_add_u32 v245, s58, v245\n"
      "v_mfma_f32_16x16x32_bf16 v[60:63], a[4:7], a[44:47], v[60:63]\n"
      "s_waitcnt vmcnt(14) lgkmcnt(0)\n"
      "s_barrier\n"
      "v_mfma_f32_16x16x32_bf16 v[16:19], a[8:11], a[32:35], v[16:19]\n"
      "ds_read_b64_tr_b16 a[0:1], v244 offset:0\n"
      "v_mfma_f32_16x16x32_bf16 v[24:27], a[8:11], a[36:39], v[24:27]\n"
      "ds_read_b64_tr_b16 a[2:3], v244 offset:3328\n"
      "buffer_load_dwordx4 v254, s[8:11], s28 offen offset:0 lds\n"
      "v_mfma_f32_16x16x32_bf16 v[64:67], a[8:11], a[40:43], v[64:67]\n"
      "ds_read_b64_tr_b16 a[4:5], v244 offset:8\n"
      "v_mfma_f32_16x16x32_bf16 v[72:75], a[8:11], a[44:47], v[72:75]\n"
      "ds_read_b64_tr_b16 a[6:7], v244 offset:3336\n"
      "v_mfma_f32_16x16x32_bf16 v[20:23], a[12:15], a[32:35], v[20:23]\n"
      "ds_read_b64_tr_b16 a[8:9], v244 offset:64\n"
      "v_mfma_f32_16x16x32_bf16 v[28:31], a[12:15], a[36:39], v[28:31]\n"
      "ds_read_b64_tr_b16 a[10:11], v244 offset:3392\n"
      "buffer_load_dwordx4 v254, s[8:11], s28 offen offset:1024 lds\n"
      "v_mfma_f32_16x16x32_bf16 v[68:71], a[12:15], a[40:43], v[68:71]\n"
      "ds_read_b128 a[48:51], v246 offset:28672\n"
      "v_mfma_f32_16x16x32_bf16 v[76:79], a[12:15], a[44:47], v[76:79]\n"
      "ds_read_b128 a[52:55], v247 offset:28672\n"
      "v_mfma_f32_16x16x32_bf16 v[32:35], a[16:19], a[32:35], v[32:35]\n"
      "ds_read_b64_tr_b16 a[12:13], v244 offset:72\n"
      "buffer_load_dwordx4 v254, s[8:11], s28 offen offset:2048 lds\n"
      "v_mfma_f32_16x16x32_bf16 v[40:43], a[16:19], a[36:39], v[40:43]\n"
      "ds_read_b64_tr_b16 a[14:15], v244 offset:3400\n"
      "v_mfma_f32_16x16x32_bf16 v[80:83], a[16:19], a[40:43], v[80:83]\n"
      "ds_read_b128 a[56:59], v248 offset:28672\n"
      "v_mfma_f32_16x16x32_bf16 v[88:91], a[16:19], a[44:47], v[88:91]\n"
      "ds_read_b128 a[60:63], v249 offset:28672\n"
      "buffer_load_dwordx4 v254, s[8:11], s28 offen offset:3072 lds\n"
      "v_mfma_f32_16x16x32_bf16 v[36:39], a[20:23], a[32:35], v[36:39]\n"
      "ds_read_b64_tr_b16 a[16:17], v244 offset:128\n"
      "v_mfma_f32_16x16x32_bf16 v[44:47], a[20:23], a[36:39], v[44:47]\n"
      "ds_read_b64_tr_b16 a[18:19], v244 offset:3456\n"
      "v_mfma_f32_16x16x32_bf16 v[84:87], a[20:23], a[40:43], v[84:87]\n"
      "s_add_u32 m0, m0, 4096\n"
      "v_mfma_f32_16x16x32_bf16 v[92:95], a[20:23], a[44:47], v[92:95]\n"
      "buffer_load_dwordx4 v254, s[52:55], s28 offen offset:0 lds\n"
      "v_mfma_f32_16x16x32_bf16 v[96:99], a[24:27], a[32:35], v[96:99]\n"
      "ds_read_b64_tr_b16 a[20:21], v244 offset:136\n"
      "v_mfma_f32_16x16x32_bf16 v[100:103], a[28:31], a[32:35], v[100:103]\n"
      "ds_read_b64_tr_b16 a[22:23], v244 offset:3464\n"
      "s_waitcnt lgkmcnt(4)\n"
      "v_mfma_f32_16x16x32_bf16 v[0:3], a[0:3], a[48:51], v[0:3]\n"
      "ds_read_b64_tr_b16 a[24:25], v245 offset:0\n"
      "v_mfma_f32_16x16x32_bf16 v[8:11], a[0:3], a[52:55], v[8:11]\n"
      "ds_read_b64_tr_b16 a[26:27], v245 offset:3328\n"
      "buffer_load_dwordx4 v254, s[52:55], s28 offen offset:1024 lds\n"
      "v_mfma_f32_16x16x32_bf16 v[48:51], a[0:3], a[56:59], v[48:51]\n"
      "ds_read_b64_tr_b16 a[28:29], v245 offset:8\n"
      "v_mfma_f32_16x16x32_bf16 v[56:59], a[0:3], a[60:63], v[56:59]\n"
      "ds_read_b64_tr_b16 a[30:31], v245 offset:3336\n"
      "v_mfma_f32_16x16x32_bf16 v[4:7], a[4:7], a[48:51], v[4:7]\n"
      "s_mov_b64 exec, s[30:31]\n"
      "buffer_load_dwordx4 v254, s[52:55], s28 offen offset:2048 lds\n"
      "s_mov_b64 exec, -1\n"
      "v_mfma_f32_16x16x32_bf16 v[12:15], a[4:7], a[52:55], v[12:15]\n"
      "s_add_u32 s28, s60, 292864\n"
      "s_mov_b32 m0, s27\n"
      "v_mfma_f32_16x16x32_bf16 v[52:55], a[4:7], a[56:59], v[52:55]\n"
      "v_add_u32 v244, s59, v244\n"
      "v_add_u32 v245, s59, v245\n"
      "v_mfma_f32_16x16x32_bf16 v[60:63], a[4:7], a[60:63], v[60:63]\n"
      "s_waitcnt vmcnt(14) lgkmcnt(0)\n"
      "s_barrier\n"
      "v_mfma_f32_16x16x32_bf16 v[16:19], a[8:11], a[48:51], v[16:19]\n"
      "ds_read_b64_tr_b16 a[0:1], v244 offset:0\n"
      "v_mfma_f32_16x16x32_bf16 v[24:27], a[8:11], a[52:55], v[24:27]\n"
      "ds_read_b64_tr_b16 a[2:3], v244 offset:3328\n"
      "buffer_load_dwordx4 v254, s[8:11], s28 offen offset:0 lds\n"
      "v_mfma_f32_16x16x32_bf16 v[64:67], a[8:11], a[56:59], v[64:67]\n"
      "ds_read_b64_tr_b16 a[4:5], v244 offset:8\n"
      "v_mfma_f32_16x16x32_bf16 v[72:75], a[8:11], a[60:63], v[72:75]\n"
      "ds_read_b64_tr_b16 a[6:7], v244 offset:3336\n"
      "v_mfma_f32_16x16x32_bf16 v[20:23], a[12:15], a[48:51], v[20:23]\n"
      "ds_read_b64_tr_b16 a[8:9], v244 offset:64\n"
      "v_mfma_f32_16x16x32_bf16 v[28:31], a[12:15], a[52:55], v[28:31]\n"
      "ds_read_b64_tr_b16 a[10:11], v244 offset:3392\n"
      "buffer_load_dwordx4 v254, s[8:11], s28 offen offset:1024 lds\n"
      "v_mfma_f32_16x16x32_bf16 v[68:71], a[12:15], a[56:59], v[68:71]\n"
      "ds_read_b128 a[32:35], v246 offset:32768\n"
      "v_mfma_f32_16x16x32_bf16 v[76:79], a[12:15], a[60:63], v[76:79]\n"
      "ds_read_b128 a[36:39], v247 offset:32768\n"
      "v_mfma_f32_16x16x32_bf16 v[32:35], a[16:19], a[48:51], v[32:35]\n"
      "ds_read_b64_tr_b16 a[12:13], v244 offset:72\n"
      "buffer_load_dwordx4 v254, s[8:11], s28 offen offset:2048 lds\n"
      "v_mfma_f32_16x16x32_bf16 v[40:43], a[16:19], a[52:55], v[40:43]\n"
      "ds_read_b64_tr_b16 a[14:15], v244 offset:3400\n"
      "v_mfma_f32_16x16x32_bf16 v[80:83], a[16:19], a[56:59], v[80:83]\n"
      "ds_read_b128 a[40:43], v248 offset:32768\n"
      "v_mfma_f32_16x16x32_bf16 v[88:91], a[16:19], a[60:63], v[88:91]\n"
      "ds_read_b128 a[44:47], v249 offset:32768\n"
      "buffer_load_dwordx4 v254, s[8:11], s28 offen offset:3072 lds\n"
      "v_mfma_f32_16x16x32_bf16 v[36:39], a[20:23], a[48:51], v[36:39]\n"
      "ds_read_b64_tr_b16 a[16:17], v244 offset:128\n"
      "v_mfma_f32_16x16x32_bf16 v[44:47], a[20:23], a[52:55], v[44:47]\n"
      "ds_read_b64_tr_b16 a[18:19], v244 offset:3456\n"
      "v_mfma_f32_16x16x32_bf16 v[84:87], a[20:23], a[56:59], v[84:87]\n"
      "s_add_u32 m0, m0, 4096\n"
      "v_mfma_f32_16x16x32_bf16 v[92:95], a[20:23], a[60:63], v[92:95]\n"
      "buffer_load_dwordx4 v254, s[52:55], s28 offen offset:0 lds\n"
      "v_mfma_f32_16x16x32_bf16 v[96:99], a[24:27], a[48:51], v[96:99]\n"
      "ds_read_b64_tr_b16 a[20:21], v244 offset:136\n"
      "v_mfma_f32_16x16x32_bf16 v[100:103], a[28:31], a[48:51], v[100:103]\n"
      "ds_read_b64_tr_b16 a[22:23], v244 offset:3464\n"
      "s_waitcnt lgkmcnt(4)\n"
      "v_mfma_f32_16x16x32_bf16 v[0:3], a[0:3], a[32:35], v[0:3]\n"
      "ds_read_b64_tr_b16 a[24:25], v245 offset:0\n"
      "v_mfma_f32_16x16x32_bf16 v[8:11], a[0:3], a[36:39], v[8:11]\n"
      "ds_read_b64_tr_b16 a[26:27], v245 offset:3328\n"
      "buffer_load_dwordx4 v254, s[52:55], s28 offen offset:1024 lds\n"
      "v_mfma_f32_16x16x32_bf16 v[48:51], a[0:3], a[40:43], v[48:51]\n"
      "ds_read_b64_tr_b16 a[28:29], v245 offset:8\n"
      "v_mfma_f32_16x16x32_bf16 v[56:59], a[0:3], a[44:47], v[56:59]\n"
      "ds_read_b64_tr_b16 a[30:31], v245 offset:3336\n"
      "v_mfma_f32_16x16x32_bf16 v[4:7], a[4:7], a[32:35], v[4:7]\n"
      "s_mov_b64 exec, s[30:31]\n"
      "buffer_load_dwordx4 v254, s[52:55], s28 offen offset:2048 lds\n"
      "s_mov_b64 exec, -1\n"
      "v_mfma_f32_16x16x32_bf16 v[12:15], a[4:7], a[36:39], v[12:15]\n"
      "s_add_u32 s28, s60, 319488\n"
      "s_mov_b32 m0, s24\n"
      "v_mfma_f32_16x16x32_bf16 v[52:55], a[4:7], a[40:43], v[52:55]\n"
      "v_add_u32 v244, s56, v244\n"
      "v_add_u32 v245, s56, v245\n"
      "v_mfma_f32_16x16x32_bf16 v[60:63], a[4:7], a[44:47], v[60:63]\n"
      "s_waitcnt vmcnt(14) lgkmcnt(0)\n"
      "s_barrier\n"
      "v_mfma_f32_16x16x32_bf16 v[16:19], a[8:11], a[32:35], v[16:19]\n"
      "ds_read_b64_tr_b16 a[0:1], v244 offset:0\n"
      "v_accvgpr_read_b32 v208, a95\n"
      "v_mfma_f32_16x16x32_bf16 v[24:27], a[8:11], a[36:39], v[24:27]\n"
      "ds_read_b64_tr_b16 a[2:3], v244 offset:3328\n"
      "buffer_load_dwordx4 v254, s[8:11], s28 offen offset:0 lds\n"
      "v_mfma_f32_16x16x32_bf16 v[64:67], a[8:11], a[40:43], v[64:67]\n"
      "ds_read_b64_tr_b16 a[4:5], v244 offset:8\n"
      "v_accvgpr_read_b32 v209, a96\n"
      "v_mfma_f32_16x16x32_bf16 v[72:75], a[8:11], a[44:47], v[72:75]\n"
      "ds_read_b64_tr_b16 a[6:7], v244 offset:3336\n"
      "ds_read_u16 a80, v208 offset:0\n"
      "v_mfma_f32_16x16x32_bf16 v[20:23], a[12:15], a[32:35], v[20:23]\n"
      "ds_read_b64_tr_b16 a[8:9], v244 offset:64\n"
      "ds_read_u16 a82, v208 offset:64\n"
      "v_mfma_f32_16x16x32_bf16 v[28:31], a[12:15], a[36:39], v[28:31]\n"
      "ds_read_b64_tr_b16 a[10:11], v244 offset:3392\n"
      "buffer_load_dwordx4 v254, s[8:11], s28 offen offset:1024 lds\n"
      "v_mfma_f32_16x16x32_bf16 v[68:71], a[12:15], a[40:43], v[68:71]\n"
      "ds_read_b128 a[48:51], v246 offset:36864\n"
      "ds_read_u16 a84, v208 offset:128\n"
      "v_mfma_f32_16x16x32_bf16 v[76:79], a[12:15], a[44:47], v[76:79]\n"
      "ds_read_b128 a[52:55], v247 offset:36864\n"
      "ds_read_u16 a86, v209 offset:0\n"
      "v_mfma_f32_16x16x32_bf16 v[32:35], a[16:19], a[32:35], v[32:35]\n"
      "ds_read_b64_tr_b16 a[12:13], v244 offset:72\n"
      "buffer_load_dwordx4 v254, s[8:11], s28 offen offset:2048 lds\n"
      "v_mfma_f32_16x16x32_bf16 v[40:43], a[16:19], a[36:39], v[40:43]\n"
      "ds_read_b64_tr_b16 a[14:15], v244 offset:3400\n"
      "v_mfma_f32_16x16x32_bf16 v[80:83], a[16:19], a[40:43], v[80:83]\n"
      "ds_read_b128 a[56:59], v248 offset:36864\n"
      "v_mfma_f32_16x16x32_bf16 v[88:91], a[16:19], a[44:47], v[88:91]\n"
      "ds_read_b128 a[60:63], v249 offset:36864\n"
      "buffer_load_dwordx4 v254, s[8:11], s28 offen offset:3072 lds\n"
      "v_mfma_f32_16x16x32_bf16 v[36:39], a[20:23], a[32:35], v[36:39]\n"
      "ds_read_b64_tr_b16 a[16:17], v244 offset:128\n"
      "v_mfma_f32_16x16x32_bf16 v[44:47], a[20:23], a[36:39], v[44:47]\n"
      "ds_read_b64_tr_b16 a[18:19], v244 offset:3456\n"
      "v_mfma_f32_16x16x32_bf16 v[84:87], a[20:23], a[40:43], v[84:87]\n"
      "s_add_u32 m0, m0, 4096\n"
      "v_mfma_f32_16x16x32_bf16 v[92:95], a[20:23], a[44:47], v[92:95]\n"
      "buffer_load_dwordx4 v254, s[52:55], s28 offen offset:0 lds\n"
      "v_mfma_f32_16x16x32_bf16 v[96:99], a[24:27], a[32:35], v[96:99]\n"
      "ds_read_b64_tr_b16 a[20:21], v244 offset:136\n"
      "v_mfma_f32_16x16x32_bf16 v[100:103], a[28:31], a[32:35], v[100:103]\n"
      "ds_read_b64_tr_b16 a[22:23], v244 offset:3464\n"
      "s_waitcnt lgkmcnt(4)\n"
      "v_mfma_f32_16x16x32_bf16 v[0:3], a[0:3], a[48:51], v[0:3]\n"
      "ds_read_b64_tr_b16 a[24:25], v245 offset:0\n"
      "v_mfma_f32_16x16x32_bf16 v[8:11], a[0:3], a[52:55], v[8:11]\n"
      "ds_read_b64_tr_b16 a[26:27], v245 offset:3328\n"
      "buffer_load_dwordx4 v254, s[52:55], s28 offen offset:1024 lds\n"
      "v_mfma_f32_16x16x32_bf16 v[48:51], a[0:3], a[56:59], v[48:51]\n"
      "ds_read_b64_tr_b16 a[28:29], v245 offset:8\n"
      "v_mfma_f32_16x16x32_bf16 v[56:59], a[0:3], a[60:63], v[56:59]\n"
      "ds_read_b64_tr_b16 a[30:31], v245 offset:3336\n"
      "v_mfma_f32_16x16x32_bf16 v[4:7], a[4:7], a[48:51], v[4:7]\n"
      "s_mov_b64 exec, s[30:31]\n"
      "buffer_load_dwordx4 v254, s[52:55], s28 offen offset:2048 lds\n"
      "s_mov_b64 exec, -1\n"
      "v_mfma_f32_16x16x32_bf16 v[12:15], a[4:7], a[52:55], v[12:15]\n"
      "s_add_u32 s28, s60, 346112\n"
      "s_mov_b32 m0, s25\n"
      "v_mfma_f32_16x16x32_bf16 v[52:55], a[4:7], a[56:59], v[52:55]\n"
      "v_add_u32 v244, s57, v244\n"
      "v_add_u32 v245, s57, v245\n"
      "v_mfma_f32_16x16x32_bf16 v[60:63], a[4:7], a[60:63], v[60:63]\n"
      "s_waitcnt vmcnt(14) lgkmcnt(0)\n"
      "s_barrier\n"
      "v_mfma_f32_16x16x32_bf16 v[16:19], a[8:11], a[48:51], v[16:19]\n"
      "ds_read_b64_tr_b16 a[0:1], v244 offset:0\n"
      "v_mfma_f32_16x16x32_bf16 v[24:27], a[8:11], a[52:55], v[24:27]\n"
      "ds_read_b64_tr_b16 a[2:3], v244 offset:3328\n"
      "buffer_load_dwordx4 v254, s[8:11], s28 offen offset:0 lds\n"
      "v_mfma_f32_16x16x32_bf16 v[64:67], a[8:11], a[56:59], v[64:67]\n"
      "ds_read_b64_tr_b16 a[4:5], v244 offset:8\n"
      "v_mfma_f32_16x16x32_bf16 v[72:75], a[8:11], a[60:63], v[72:75]\n"
      "ds_read_b64_tr_b16 a[6:7], v244 offset:3336\n"
      "v_mfma_f32_16x16x32_bf16 v[20:23], a[12:15], a[48:51], v[20:23]\n"
      "ds_read_b64_tr_b16 a[8:9], v244 offset:64\n"
      "v_mfma_f32_16x16x32_bf16 v[28:31], a[12:15], a[52:55], v[28:31]\n"
      "ds_read_b64_tr_b16 a[10:11], v244 offset:3392\n"
      "buffer_load_dwordx4 v254, s[8:11], s28 offen offset:1024 lds\n"
      "v_mfma_f32_16x16x32_bf16 v[68:71], a[12:15], a[56:59], v[68:71]\n"
      "ds_read_b128 a[32:35], v246 offset:40960\n"
      "v_mfma_f32_16x16x32_bf16 v[76:79], a[12:15], a[60:63], v[76:79]\n"
      "ds_read_b128 a[36:39], v247 offset:40960\n"
      "v_mfma_f32_16x16x32_bf16 v[32:35], a[16:19], a[48:51], v[32:35]\n"
      "ds_read_b64_tr_b16 a[12:13], v244 offset:72\n"
      "buffer_load_dwordx4 v254, s[8:11], s28 offen offset:2048 lds\n"
      "v_mfma_f32_16x16x32_bf16 v[40:43], a[16:19], a[52:55], v[40:43]\n"
      "ds_read_b64_tr_b16 a[14:15], v244 offset:3400\n"
      "v_mfma_f32_16x16x32_bf16 v[80:83], a[16:19], a[56:59], v[80:83]\n"
      "ds_read_b128 a[40:43], v248 offset:40960\n"
      "v_mfma_f32_16x16x32_bf16 v[88:91], a[16:19], a[60:63], v[88:91]\n"
      "ds_read_b128 a[44:47], v249 offset:40960\n"
      "buffer_load_dwordx4 v254, s[8:11], s28 offen offset:3072 lds\n"
      "v_mfma_f32_16x16x32_bf16 v[36:39], a[20:23], a[48:51], v[36:39]\n"
      "ds_read_b64_tr_b16 a[16:17], v244 offset:128\n"
      "v_mfma_f32_16x16x32_bf16 v[44:47], a[20:23], a[52:55], v[44:47]\n"
      "ds_read_b64_tr_b16 a[18:19], v244 offset:3456\n"
      "v_mfma_f32_16x16x32_bf16 v[84:87], a[20:23], a[56:59], v[84:87]\n"
      "s_add_u32 m0, m0, 4096\n"
      "v_mfma_f32_16x16x32_bf16 v[92:95], a[20:23], a[60:63], v[92:95]\n"
      "buffer_load_dwordx4 v254, s[52:55], s28 offen offset:0 lds\n"
      "v_mfma_f32_16x16x32_bf16 v[96:99], a[24:27], a[48:51], v[96:99]\n"
      "ds_read_b64_tr_b16 a[20:21], v244 offset:136\n"
      "v_mfma_f32_16x16x32_bf16 v[100:103], a[28:31], a[48:51], v[100:103]\n"
      "ds_read_b64_tr_b16 a[22:23], v244 offset:3464\n"
      "s_waitcnt lgkmcnt(4)\n"
      "s_sub_u32 s29, s29, 1\n"
      "s_cmp_eq_u32 s29, 0\n"
      "s_cbranch_scc1 LFIN_%=\n"
      "v_mfma_f32_16x16x32_bf16 v[0:3], a[0:3], a[32:35], v[0:3]\n"
      "ds_read_b64_tr_b16 a[24:25], v245 offset:0\n"
      "v_mfma_f32_16x16x32_bf16 v[8:11], a[0:3], a[36:39], v[8:11]\n"
      "ds_read_b64_tr_b16 a[26:27], v245 offset:3328\n"
      "buffer_load_dwordx4 v254, s[52:55], s28 offen offset:1024 lds\n"
      "v_mfma_f32_16x16x32_bf16 v[48:51], a[0:3], a[40:43], v[48:51]\n"
      "ds_read_b64_tr_b16 a[28:29], v245 offset:8\n"
      "v_mfma_f32_16x16x32_bf16 v[56:59], a[0:3], a[44:47], v[56:59]\n"
      "ds_read_b64_tr_b16 a[30:31], v245 offset:3336\n"
      "v_mfma_f32_16x16x32_bf16 v[4:7], a[4:7], a[32:35], v[4:7]\n"
      "s_mov_b64 exec, s[30:31]\n"
      "buffer_load_dwordx4 v254, s[52:55], s28 offen offset:2048 lds\n"
      "s_mov_b64 exec, -1\n"
      "v_mfma_f32_16x16x32_bf16 v[12:15], a[4:7], a[36:39], v[12:15]\n"
      "s_add_u32 s28, s60, 372736\n"
      "s_mov_b32 m0, s26\n"
      "v_mfma_f32_16x16x32_bf16 v[52:55], a[4:7], a[40:43], v[52:55]\n"
      "v_add_u32 v244, s58, v244\n"
      "v_add_u32 v245, s58, v245\n"
      "v_mfma_f32_16x16x32_bf16 v[60:63], a[4:7], a[44:47], v[60:63]\n"
      "s_waitcnt vmcnt(14) lgkmcnt(0)\n"
      "s_barrier\n"
      "v_mfma_f32_16x16x32_bf16 v[16:19], a[8:11], a[32:35], v[16:19]\n"
      "ds_read_b64_tr_b16 a[0:1], v244 offset:0\n"
      "v_mfma_f32_16x16x32_bf16 v[24:27], a[8:11], a[36:39], v[24:27]\n"
      "ds_read_b64_tr_b16 a[2:3], v244 offset:3328\n"
      "buffer_load_dwordx4 v254, s[8:11], s28 offen offset:0 lds\n"
      "v_mfma_f32_16x16x32_bf16 v[64:67], a[8:11], a[40:43], v[64:67]\n"
      "ds_read_b64_tr_b16 a[4:5], v244 offset:8\n"
      "v_mfma_f32_16x16x32_bf16 v[72:75], a[8:11], a[44:47], v[72:75]\n"
      "ds_read_b64_tr_b16 a[6:7], v244 offset:3336\n"
      "v_mfma_f32_16x16x32_bf16 v[20:23], a[12:15], a[32:35], v[20:23]\n"
      "ds_read_b64_tr_b16 a[8:9], v244 offset:64\n"
      "v_mfma_f32_16x16x32_bf16 v[28:31], a[12:15], a[36:39], v[28:31]\n"
      "ds_read_b64_tr_b16 a[10:11], v244 offset:3392\n"
      "buffer_load_dwordx4 v254, s[8:11], s28 offen offset:1024 lds\n"
      "v_mfma_f32_16x16x32_bf16 v[68:71], a[12:15], a[40:43], v[68:71]\n"
      "ds_read_b128 a[48:51], v246 offset:45056\n"
      "v_mfma_f32_16x16x32_bf16 v[76:79], a[12:15], a[44:47], v[76:79]\n"
      "ds_read_b128 a[52:55], v247 offset:45056\n"
      "v_mfma_f32_16x16x32_bf16 v[32:35], a[16:19], a[32:35], v[32:35]\n"
      "ds_read_b64_tr_b16 a[12:13], v244 offset:72\n"
      "buffer_load_dwordx4 v254, s[8:11], s28 offen offset:2048 lds\n"
      "v_mfma_f32_16x16x32_bf16 v[40:43], a[16:19], a[36:39], v[40:43]\n"
      "ds_read_b64_tr_b16 a[14:15], v244 offset:3400\n"
      "v_mfma_f32_16x16x32_bf16 v[80:83], a[16:19], a[40:43], v[80:83]\n"
      "ds_read_b128 a[56:59], v248 offset:45056\n"
      "v_mfma_f32_16x16x32_bf16 v[88:91], a[16:19], a[44:47], v[88:91]\n"
      "ds_read_b128 a[60:63], v249 offset:45056\n"
      "buffer_load_dwordx4 v254, s[8:11], s28 offen offset:3072 lds\n"
      "v_mfma_f32_16x16x32_bf16 v[36:39], a[20:23], a[32:35], v[36:39]\n"
      "ds_read_b64_tr_b16 a[16:17], v244 offset:128\n"
      "v_mfma_f32_16x16x32_bf16 v[44:47], a[20:23], a[36:39], v[44:47]\n"
      "ds_read_b64_tr_b16 a[18:19], v244 offset:3456\n"
      "v_mfma_f32_16x16x32_bf16 v[84:87], a[20:23], a[40:43], v[84:87]\n"
      "s_add_u32 m0, m0, 4096\n"
      "v_mfma_f32_16x16x32_bf16 v[92:95], a[20:23], a[44:47], v[92:95]\n"
      "buffer_load_dwordx4 v254, s[52:55], s28 offen offset:0 lds\n"
      "v_mfma_f32_16x16x32_bf16 v[96:99], a[24:27], a[32:35], v[96:99]\n"
      "ds_read_b64_tr_b16 a[20:21], v244 offset:136\n"
      "v_mfma_f32_16x16x32_bf16 v[100:103], a[28:31], a[32:35], v[100:103]\n"
      "ds_read_b64_tr_b16 a[22:23], v244 offset:3464\n"
      "s_waitcnt lgkmcnt(4)\n"
      "v_mfma_f32_16x16x32_bf16 v[0:3], a[0:3], a[48:51], v[0:3]\n"
      "ds_read_b64_tr_b16 a[24:25], v245 offset:0\n"
      "v_mfma_f32_16x16x32_bf16 v[8:11], a[0:3], a[52:55], v[8:11]\n"
      "ds_read_b64_tr_b16 a[26:27], v245 offset:3328\n"
      "buffer_load_dwordx4 v254, s[52:55], s28 offen offset:1024 lds\n"
      "v_mfma_f32_16x16x32_bf16 v[48:51], a[0:3], a[56:59], v[48:51]\n"
      "ds_read_b64_tr_b16 a[28:29], v245 offset:8\n"
      "v_mfma_f32_16x16x32_bf16 v[56:59], a[0:3], a[60:63], v[56:59]\n"
      "ds_read_b64_tr_b16 a[30:31], v245 offset:3336\n"
      "v_mfma_f32_16x16x32_bf16 v[4:7], a[4:7], a[48:51], v[4:7]\n"
      "s_mov_b64 exec, s[30:31]\n"
      "buffer_load_dwordx4 v254, s[52:55], s28 offen offset:2048 lds\n"
      "s_mov_b64 exec, -1\n"
      "v_mfma_f32_16x16x32_bf16 v[12:15], a[4:7], a[52:55], v[12:15]\n"
      "s_add_u32 s28, s60, 399360\n"
      "s_mov_b32 m0, s27\n"
      "v_mfma_f32_16x16x32_bf16 v[52:55], a[4:7], a[56:59], v[52:55]\n"
      "v_add_u32 v244, s59, v244\n"
      "v_add_u32 v245, s59, v245\n"
      "v_mfma_f32_16x16x32_bf16 v[60:63], a[4:7], a[60:63], v[60:63]\n"
      "s_waitcnt vmcnt(14) lgkmcnt(0)\n"
      "s_barrier\n"
      "v_mfma_f32_16x16x32_bf16 v[16:19], a[8:11], a[48:51], v[16:19]\n"
      "ds_read_b64_tr_b16 a[0:1], v244 offset:0\n"
      "v_mfma_f32_16x16x32_bf16 v[24:27], a[8:11], a[52:55], v[24:27]\n"
      "ds_read_b64_tr_b16 a[2:3], v244 offset:3328\n"
      "buffer_load_dwordx4 v254, s[8:11], s28 offen offset:0 lds\n"
      "v_mfma_f32_16x16x32_bf16 v[64:67], a[8:11], a[56:59], v[64:67]\n"
      "ds_read_b64_tr_b16 a[4:5], v244 offset:8\n"
      "v_mfma_f32_16x16x32_bf16 v[72:75], a[8:11], a[60:63], v[72:75]\n"
      "ds_read_b64_tr_b16 a[6:7], v244 offset:3336\n"
      "v_mfma_f32_16x16x32_bf16 v[20:23], a[12:15], a[48:51], v[20:23]\n"
      "ds_read_b64_tr_b16 a[8:9], v244 offset:64\n"
      "v_mfma_f32_16x16x32_bf16 v[28:31], a[12:15], a[52:55], v[28:31]\n"
      "ds_read_b64_tr_b16 a[10:11], v244 offset:3392\n"
      "buffer_load_dwordx4 v254, s[8:11], s28 offen offset:1024 lds\n"
      "v_mfma_f32_16x16x32_bf16 v[68:71], a[12:15], a[56:59], v[68:71]\n"
      "ds_read_b128 a[32:35], v246 offset:49152\n"
      "v_mfma_f32_16x16x32_bf16 v[76:79], a[12:15], a[60:63], v[76:79]\n"
      "ds_read_b128 a[36:39], v247 offset:49152\n"
      "v_mfma_f32_16x16x32_bf16 v[32:35], a[16:19], a[48:51], v[32:35]\n"
      "ds_read_b64_tr_b16 a[12:13], v244 offset:72\n"
      "buffer_load_dwordx4 v254, s[8:11], s28 offen offset:2048 lds\n"
      "v_mfma_f32_16x16x32_bf16 v[40:43], a[16:19], a[52:55], v[40:43]\n"
      "ds_read_b64_tr_b16 a[14:15], v244 offset:3400\n"
      "v_mfma_f32_16x16x32_bf16 v[80:83], a[16:19], a[56:59], v[80:83]\n"
      "ds_read_b128 a[40:43], v248 offset:49152\n"
      "v_mfma_f32_16x16x32_bf16 v[88:91], a[16:19], a[60:63], v[88:91]\n"
      "ds_read_b128 a[44:47], v249 offset:49152\n"
      "buffer_load_dwordx4 v254, s[8:11], s28 offen offset:3072 lds\n"
      "v_mfma_f32_16x16x32_bf16 v[36:39], a[20:23], a[48:51], v[36:39]\n"
      "ds_read_b64_tr_b16 a[16:17], v244 offset:128\n"
      "v_mfma_f32_16x16x32_bf16 v[44:47], a[20:23], a[52:55], v[44:47]\n"
      "ds_read_b64_tr_b16 a[18:19], v244 offset:3456\n"
      "v_mfma_f32_16x16x32_bf16 v[84:87], a[20:23], a[56:59], v[84:87]\n"
      "s_add_u32 m0, m0, 4096\n"
      "v_mfma_f32_16x16x32_bf16 v[92:95], a[20:23], a[60:63], v[92:95]\n"
      "buffer_load_dwordx4 v254, s[52:55], s28 offen offset:0 lds\n"
      "v_mfma_f32_16x16x32_bf16 v[96:99], a[24:27], a[48:51], v[96:99]\n"
      "ds_read_b64_tr_b16 a[20:21], v244 offset:136\n"
      "v_mfma_f32_16x16x32_bf16 v[100:103], a[28:31], a[48:51], v[100:103]\n"
      "ds_read_b64_tr_b16 a[22:23], v244 offset:3464\n"
      "s_waitcnt lgkmcnt(4)\n"
      "LS12_%=:\n"
      "v_mfma_f32_16x16x32_bf16 v[0:3], a[0:3], a[32:35], v[0:3]\n"
      "ds_read_b64_tr_b16 a[24:25], v245 offset:0\n"
      "v_mfma_f32_16x16x32_bf16 v[8:11], a[0:3], a[36:39], v[8:11]\n"
      "ds_read_b64_tr_b16 a[26:27], v245 offset:3328\n"
      "buffer_load_dwordx4 v254, s[52:55], s28 offen offset:1024 lds\n"
      "v_mfma_f32_16x16x32_bf16 v[48:51], a[0:3], a[40:43], v[48:51]\n"
      "ds_read_b64_tr_b16 a[28:29], v245 offset:8\n"
      "v_mfma_f32_16x16x32_bf16 v[56:59], a[0:3], a[44:47], v[56:59]\n"
      "ds_read_b64_tr_b16 a[30:31], v245 offset:3336\n"
      "v_mfma_f32_16x16x32_bf16 v[4:7], a[4:7], a[32:35], v[4:7]\n"
      "s_mov_b64 exec, s[30:31]\n"
      "buffer_load_dwordx4 v254, s[52:55], s28 offen offset:2048 lds\n"
      "s_mov_b64 exec, -1\n"
      "v_mfma_f32_16x16x32_bf16 v[12:15], a[4:7], a[36:39], v[12:15]\n"
      "s_add_u32 s28, s60, 425984\n"
      "s_mov_b32 m0, s24\n"
      "v_mfma_f32_16x16x32_bf16 v[52:55], a[4:7], a[40:43], v[52:55]\n"
      "v_add_u32 v244, s56, v244\n"
      "v_add_u32 v245, s56, v245\n"
      "v_mfma_f32_16x16x32_bf16 v[60:63], a[4:7], a[44:47], v[60:63]\n"
      "s_waitcnt vmcnt(14) lgkmcnt(0)\n"
      "s_barrier\n"
      "v_mfma_f32_16x16x32_bf16 v[16:19], a[8:11], a[32:35], v[16:19]\n"
      "ds_read_b64_tr_b16 a[0:1], v244 offset:0\n"
      "v_mfma_f32_16x16x32_bf16 v[24:27], a[8:11], a[36:39], v[24:27]\n"
      "ds_read_b64_tr_b16 a[2:3], v244 offset:3328\n"
      "buffer_load_dwordx4 v254, s[8:11], s28 offen offset:0 lds\n"
      "v_pk_mul_f32 v[208:209], v[0:1], v[104:105]\n"
      "v_pk_mul_f32 v[210:211], v[2:3], v[106:107]\n"
      "v_pk_mul_f32 v[212:213], v[4:5], v[108:109]\n"
      "v_pk_mul_f32 v[214:215], v[6:7], v[110:111]\n"
      "v_mfma_f32_16x16x32_bf16 v[64:67], a[8:11], a[40:43], v[64:67]\n"
      "ds_read_b64_tr_b16 a[4:5], v244 offset:8\n"
      "v_mfma_f32_16x16x32_bf16 v[72:75], a[8:11], a[44:47], v[72:75]\n"
      "ds_read_b64_tr_b16 a[6:7], v244 offset:3336\n"
      "v_cvt_pk_bf16_f32 v216, v208, v209\n"
      "v_cvt_pk_bf16_f32 v217, v210, v211\n"
      "v_cvt_pk_bf16_f32 v218, v212, v213\n"
      "v_cvt_pk_bf16_f32 v219, v214, v215\n"
      "ds_write_b128 v250, v[216:219] offset:0\n"
      "v_pk_mul_f32 v[208:209], v[8:9], v[112:113]\n"
      "v_pk_mul_f32 v[210:211], v[10:11], v[114:115]\n"
      "v_pk_mul_f32 v[212:213], v[12:13], v[116:117]\n"
      "v_pk_mul_f32 v[214:215], v[14:15], v[118:119]\n"
      "v_mfma_f32_16x16x4_4b_bf16 v[0:15], a[80:81], a[88:89], v[0:15]\n"
      "v_cvt_pk_bf16_f32 v220, v208, v209\n"
      "v_cvt_pk_bf16_f32 v221, v210, v211\n"
      "v_cvt_pk_bf16_f32 v222, v212, v213\n"
      "v_cvt_pk_bf16_f32 v223, v214, v215\n"
      "ds_write_b128 v251, v[220:223] offset:0\n"
      "v_pk_mul_f32 v[208:209], v[48:49], v[152:153]\n"
      "v_pk_mul_f32 v[210:211], v[50:51], v[154:155]\n"
      "v_pk_mul_f32 v[212:213], v[52:53], v[156:157]\n"
      "v_pk_mul_f32 v[214:215], v[54:55], v[158:159]\n"
      "v_mfma_f32_16x16x32_bf16 v[20:23], a[12:15], a[32:35], v[20:23]\n"
      "ds_read_b64_tr_b16 a[8:9], v244 offset:64\n"
      "v_mfma_f32_16x16x32_bf16 v[28:31], a[12:15], a[36:39], v[28:31]\n"
      "ds_read_b64_tr_b16 a[10:11], v244 offset:3392\n"
      "buffer_load_dwordx4 v254, s[8:11], s28 offen offset:1024 lds\n"
      "v_cvt_pk_bf16_f32 v224, v208, v209\n"
      "v_cvt_pk_bf16_f32 v225, v210, v211\n"
      "v_cvt_pk_bf16_f32 v226, v212, v213\n"
      "v_cvt_pk_bf16_f32 v227, v214, v215\n"
      "ds_write_b128 v252, v[224:227] offset:0\n"
      "v_pk_mul_f32 v[208:209], v[56:57], v[160:161]\n"
      "v_pk_mul_f32 v[210:211], v[58:59], v[162:163]\n"
      "v_pk_mul_f32 v[212:213], v[60:61], v[164:165]\n"
      "v_pk_mul_f32 v[214:215], v[62:63], v[166:167]\n"
      "v_mfma_f32_16x16x4_4b_bf16 v[48:63], a[80:81], a[88:89], v[48:63]\n"
      "v_cvt_pk_bf16_f32 v228, v208, v209\n"
      "v_cvt_pk_bf16_f32 v229, v210, v211\n"
      "v_cvt_pk_bf16_f32 v230, v212, v213\n"
      "v_cvt_pk_bf16_f32 v231, v214, v215\n"
      "ds_write_b128 v253, v[228:231] offset:0\n"
      "v_mfma_f32_16x16x32_bf16 v[68:71], a[12:15], a[40:43], v[68:71]\n"
      "v_mfma_f32_16x16x32_bf16 v[76:79], a[12:15], a[44:47], v[76:79]\n"
      "v_mfma_f32_16x16x32_bf16 v[32:35], a[16:19], a[32:35], v[32:35]\n"
      "ds_read_b64_tr_b16 a[12:13], v244 offset:72\n"
      "buffer_load_dwordx4 v254, s[8:11], s28 offen offset:2048 lds\n"
      "v_mfma_f32_16x16x32_bf16 v[40:43], a[16:19], a[36:39], v[40:43]\n"
      "ds_read_b64_tr_b16 a[14:15], v244 offset:3400\n"
      "v_pk_mul_f32 v[208:209], v[16:17], v[120:121]\n"
      "v_pk_mul_f32 v[210:211], v[18:19], v[122:123]\n"
      "v_pk_mul_f32 v[212:213], v[20:21], v[124:125]\n"
      "v_pk_mul_f32 v[214:215], v[22:23], v[126:127]\n"
      "v_mfma_f32_16x16x32_bf16 v[80:83], a[16:19], a[40:43], v[80:83]\n"
      "v_mfma_f32_16x16x32_bf16 v[88:91], a[16:19], a[44:47], v[88:91]\n"
      "buffer_load_dwordx4 v254, s[8:11], s28 offen offset:3072 lds\n"
      "v_cvt_pk_bf16_f32 v232, v208, v209\n"
      "v_cvt_pk_bf16_f32 v233, v210, v211\n"
      "v_cvt_pk_bf16_f32 v234, v212, v213\n"
      "v_cvt_pk_bf16_f32 v235, v214, v215\n"
      "ds_write_b128 v250, v[232:235] offset:4096\n"
      "s_waitcnt lgkmcnt(3)\n"
      "s_barrier\n"
      "ds_read_b128 a[64:67], v246 offset:0\n"
      "ds_read_b128 a[68:71], v247 offset:0\n"
      "ds_read_b128 a[72:75], v248 offset:0\n"
      "ds_read_b128 a[76:79], v249 offset:0\n"
      "v_pk_mul_f32 v[208:209], v[24:25], v[128:129]\n"
      "v_pk_mul_f32 v[210:211], v[26:27], v[130:131]\n"
      "v_pk_mul_f32 v[212:213], v[28:29], v[132:133]\n"
      "v_pk_mul_f32 v[214:215], v[30:31], v[134:135]\n"
      "v_mfma_f32_16x16x4_4b_bf16 v[16:31], a[82:83], a[88:89], v[16:31]\n"
      "v_cvt_pk_bf16_f32 v236, v208, v209\n"
      "v_cvt_pk_bf16_f32 v237, v210, v211\n"
      "v_cvt_pk_bf16_f32 v238, v212, v213\n"
      "v_cvt_pk_bf16_f32 v239, v214, v215\n"
      "ds_write_b128 v251, v[236:239] offset:4096\n"
      "v_pk_mul_f32 v[208:209], v[64:65], v[168:169]\n"
      "v_pk_mul_f32 v[210:211], v[66:67], v[170:171]\n"
      "v_pk_mul_f32 v[212:213], v[68:69], v[172:173]\n"
      "v_pk_mul_f32 v[214:215], v[70:71], v[174:175]\n"
      "v_mfma_f32_16x16x32_bf16 v[36:39], a[20:23], a[32:35], v[36:39]\n"
      "ds_read_b64_tr_b16 a[16:17], v244 offset:128\n"
      "v_mfma_f32_16x16x32_bf16 v[44:47], a[20:23], a[36:39], v[44:47]\n"
      "ds_read_b64_tr_b16 a[18:19], v244 offset:3456\n"
      "v_cvt_pk_bf16_f32 v240, v208, v209\n"
      "v_cvt_pk_bf16_f32 v241, v210, v211\n"
      "v_cvt_pk_bf16_f32 v242, v212, v213\n"
      "v_cvt_pk_bf16_f32 v243, v214, v215\n"
      "ds_write_b128 v252, v[240:243] offset:4096\n"
      "v_pk_mul_f32 v[208:209], v[72:73], v[176:177]\n"
      "v_pk_mul_f32 v[210:211], v[74:75], v[178:179]\n"
      "v_pk_mul_f32 v[212:213], v[76:77], v[180:181]\n"
      "v_pk_mul_f32 v[214:215], v[78:79], v[182:183]\n"
      "v_mfma_f32_16x16x4_4b_bf16 v[64:79], a[82:83], a[88:89], v[64:79]\n"
      "v_cvt_pk_bf16_f32 v216, v208, v209\n"
      "v_cvt_pk_bf16_f32 v217, v210, v211\n"
      "v_cvt_pk_bf16_f32 v218, v212, v213\n"
      "v_cvt_pk_bf16_f32 v219, v214, v215\n"
      "ds_write_b128 v253, v[216:219] offset:4096\n"
      "v_mfma_f32_16x16x32_bf16 v[84:87], a[20:23], a[40:43], v[84:87]\n"
      "s_add_u32 m0, m0, 4096\n"
      "v_mfma_f32_16x16x32_bf16 v[92:95], a[20:23], a[44:47], v[92:95]\n"
      "buffer_load_dwordx4 v254, s[52:55], s28 offen offset:0 lds\n"
      "v_mfma_f32_16x16x32_bf16 v[96:99], a[24:27], a[32:35], v[96:99]\n"
      "ds_read_b64_tr_b16 a[20:21], v244 offset:136\n"
      "v_mfma_f32_16x16x32_bf16 v[100:103], a[28:31], a[32:35], v[100:103]\n"
      "ds_read_b64_tr_b16 a[22:23], v244 offset:3464\n"
      "s_mov_b32 s36, s24\n"
      "s_mov_b32 s24, s25\n"
      "s_mov_b32 s25, s26\n"
      "s_mov_b32 s26, s27\n"
      "s_mov_b32 s27, s36\n"
      "s_mov_b32 s36, s56\n"
      "s_mov_b32 s56, s57\n"
      "s_mov_b32 s57, s58\n"
      "s_mov_b32 s58, s59\n"
      "s_mov_b32 s59, s36\n"
      "s_add_u32 s60, s60, 346112\n"
      "s_branch LTOP_%=\n"
      "LFIN_%=:\n"
      "v_and_b32 v208, 15, %[tid]\n"
      "v_bfe_u32 v209, %[tid], 4, 2\n"
      "v_mul_u32_u24 v208, 832, v208\n"
      "v_lshl_add_u32 v251, v209, 4, v208\n"
      "s_mul_i32 s36, s34, 192\n"
      "v_add_u32 v250, s36, v251\n"
      "v_add_u32 v212, s58, v244\n"
      "v_add_u32 v213, s58, v245\n"
      "v_add_u32 v214, s59, v212\n"
      "v_add_u32 v215, s59, v213\n"
      "v_mfma_f32_16x16x32_bf16 v[0:3], a[0:3], a[32:35], v[0:3]\n"
      "ds_read_b128 a[48:51], v246 offset:45056\n"
      "ds_read_b128 a[52:55], v247 offset:45056\n"
      "v_mfma_f32_16x16x32_bf16 v[8:11], a[0:3], a[36:39], v[8:11]\n"
      "ds_read_b128 a[56:59], v248 offset:45056\n"
      "ds_read_b128 a[60:63], v249 offset:45056\n"
      "v_mfma_f32_16x16x32_bf16 v[48:51], a[0:3], a[40:43], v[48:51]\n"
      "s_waitcnt vmcnt(12)\n"
      "s_barrier\n"
      "ds_read_b64_tr_b16 a[100:101], v212 offset:0\n"
      "ds_read_b64_tr_b16 a[102:103], v212 offset:3328\n"
      "v_mfma_f32_16x16x32_bf16 v[56:59], a[0:3], a[44:47], v[56:59]\n"
      "ds_read_b64_tr_b16 a[104:105], v212 offset:8\n"
      "ds_read_b64_tr_b16 a[106:107], v212 offset:3336\n"
      "v_mfma_f32_16x16x32_bf16 v[4:7], a[4:7], a[32:35], v[4:7]\n"
      "ds_read_b128 a[64:67], v246 offset:49152\n"
      "v_mfma_f32_16x16x32_bf16 v[12:15], a[4:7], a[36:39], v[12:15]\n"
      "ds_read_b128 a[68:71], v247 offset:49152\n"
      "v_mfma_f32_16x16x32_bf16 v[52:55], a[4:7], a[40:43], v[52:55]\n"
      "ds_read_b128 a[72:75], v248 offset:49152\n"
      "v_mfma_f32_16x16x32_bf16 v[60:63], a[4:7], a[44:47], v[60:63]\n"
      "ds_read_b128 a[76:79], v249 offset:49152\n"
      "s_waitcnt lgkmcnt(4)\n"
      "v_mfma_f32_16x16x32_bf16 v[0:3], a[100:103], a[48:51], v[0:3]\n"
      "v_mfma_f32_16x16x32_bf16 v[8:11], a[100:103], a[52:55], v[8:11]\n"
      "v_mfma_f32_16x16x32_bf16 v[48:51], a[100:103], a[56:59], v[48:51]\n"
      "s_waitcnt vmcnt(5)\n"
      "s_barrier\n"
      "ds_read_b64_tr_b16 a[132:133], v214 offset:0\n"
      "ds_read_b64_tr_b16 a[134:135], v214 offset:3328\n"
      "v_mfma_f32_16x16x32_bf16 v[56:59], a[100:103], a[60:63], v[56:59]\n"
      "ds_read_b64_tr_b16 a[136:137], v214 offset:8\n"
      "ds_read_b64_tr_b16 a[138:139], v214 offset:3336\n"
      "v_mfma_f32_16x16x32_bf16 v[4:7], a[104:107], a[48:51], v[4:7]\n"
      "ds_read_b64_tr_b16 a[108:109], v212 offset:64\n"
      "ds_read_b64_tr_b16 a[110:111], v212 offset:3392\n"
      "v_mfma_f32_16x16x32_bf16 v[12:15], a[104:107], a[52:55], v[12:15]\n"
      "ds_read_b64_tr_b16 a[112:113], v212 offset:72\n"
      "v_mfma_f32_16x16x32_bf16 v[52:55], a[104:107], a[56:59], v[52:55]\n"
      "ds_read_b64_tr_b16 a[114:115], v212 offset:3400\n"
      "v_mfma_f32_16x16x32_bf16 v[60:63], a[104:107], a[60:63], v[60:63]\n"
      "s_waitcnt lgkmcnt(4)\n"
      "ds_read_b64_tr_b16 a[140:141], v214 offset:64\n"
      "ds_read_b64_tr_b16 a[142:143], v214 offset:3392\n"
      "v_mfma_f32_16x16x32_bf16 v[0:3], a[132:135], a[64:67], v[0:3]\n"
      "ds_read_b64_tr_b16 a[144:145], v214 offset:72\n"
      "v_mfma_f32_16x16x32_bf16 v[8:11], a[132:135], a[68:71], v[8:11]\n"
      "ds_read_b64_tr_b16 a[146:147], v214 offset:3400\n"
      "v_mfma_f32_16x16x32_bf16 v[48:51], a[132:135], a[72:75], v[48:51]\n"
      "ds_read_b64_tr_b16 a[116:117], v212 offset:128\n"
      "ds_read_b64_tr_b16 a[118:119], v212 offset:3456\n"
      "v_mfma_f32_16x16x32_bf16 v[56:59], a[132:135], a[76:79], v[56:59]\n"
      "ds_read_b64_tr_b16 a[120:121], v212 offset:136\n"
      "v_mfma_f32_16x16x32_bf16 v[4:7], a[136:139], a[64:67], v[4:7]\n"
      "ds_read_b64_tr_b16 a[122:123], v212 offset:3464\n"
      "v_mfma_f32_16x16x32_bf16 v[12:15], a[136:139], a[68:71], v[12:15]\n"
      "ds_read_b64_tr_b16 a[148:149], v214 offset:128\n"
      "ds_read_b64_tr_b16 a[150:151], v214 offset:3456\n"
      "v_mfma_f32_16x16x32_bf16 v[52:55], a[136:139], a[72:75], v[52:55]\n"
      "ds_read_b64_tr_b16 a[152:153], v214 offset:136\n"
      "v_mfma_f32_16x16x32_bf16 v[60:63], a[136:139], a[76:79], v[60:63]\n"
      "ds_read_b64_tr_b16 a[154:155], v214 offset:3464\n"
      "v_mfma_f32_16x16x32_bf16 v[16:19], a[8:11], a[32:35], v[16:19]\n"
      "v_mfma_f32_16x16x32_bf16 v[24:27], a[8:11], a[36:39], v[24:27]\n"
      "s_nop 2\n"
      "v_pk_mul_f32 v[0:1], v[0:1], v[104:105]\n"
      "v_pk_mul_f32 v[2:3], v[2:3], v[106:107]\n"
      "v_pk_mul_f32 v[4:5], v[4:5], v[108:109]\n"
      "v_pk_mul_f32 v[6:7], v[6:7], v[110:111]\n"
      "v_pk_mul_f32 v[8:9], v[8:9], v[112:113]\n"
      "v_pk_mul_f32 v[10:11], v[10:11], v[114:115]\n"
      "v_pk_mul_f32 v[12:13], v[12:13], v[116:117]\n"
      "v_pk_mul_f32 v[14:15], v[14:15], v[118:119]\n"
      "v_pk_mul_f32 v[48:49], v[48:49], v[152:153]\n"
      "v_pk_mul_f32 v[50:51], v[50:51], v[154:155]\n"
      "v_pk_mul_f32 v[52:53], v[52:53], v[156:157]\n"
      "v_pk_mul_f32 v[54:55], v[54:55], v[158:159]\n"
      "v_pk_mul_f32 v[56:57], v[56:57], v[160:161]\n"
      "v_pk_mul_f32 v[58:59], v[58:59], v[162:163]\n"
      "v_pk_mul_f32 v[60:61], v[60:61], v[164:165]\n"
      "v_pk_mul_f32 v[62:63], v[62:63], v[166:167]\n"
      "v_mfma_f32_16x16x32_bf16 v[64:67], a[8:11], a[40:43], v[64:67]\n"
      "v_cvt_pk_bf16_f32 v0, v0, v1\n"
      "v_cvt_pk_bf16_f32 v1, v2, v3\n"
      "v_mfma_f32_16x16x32_bf16 v[72:75], a[8:11], a[44:47], v[72:75]\n"
      "v_cvt_pk_bf16_f32 v2, v4, v5\n"
      "v_cvt_pk_bf16_f32 v3, v6, v7\n"
      "v_mfma_f32_16x16x32_bf16 v[20:23], a[12:15], a[32:35], v[20:23]\n"
      "buffer_store_dwordx4 v[0:3], v250, s[16:19], s42 offen offset:0 sc0 sc1\n"
      "v_mfma_f32_16x16x32_bf16 v[28:31], a[12:15], a[36:39], v[28:31]\n"
      "v_cvt_pk_bf16_f32 v8, v8, v9\n"
      "v_cvt_pk_bf16_f32 v9, v10, v11\n"
      "v_mfma_f32_16x16x32_bf16 v[68:71], a[12:15], a[40:43], v[68:71]\n"
      "v_cvt_pk_bf16_f32 v10, v12, v13\n"
      "v_cvt_pk_bf16_f32 v11, v14, v15\n"
      "v_mfma_f32_16x16x32_bf16 v[76:79], a[12:15], a[44:47], v[76:79]\n"
      "s_waitcnt lgkmcnt(0)\n"
      "buffer_store_dwordx4 v[8:11], v250, s[16:19], s43 offen offset:0 sc0 sc1\n"
      "v_mfma_f32_16x16x32_bf16 v[16:19], a[108:111], a[48:51], v[16:19]\n"
      "v_cvt_pk_bf16_f32 v48, v48, v49\n"
      "v_cvt_pk_bf16_f32 v49, v50, v51\n"
      "v_mfma_f32_16x16x32_bf16 v[24:27], a[108:111], a[52:55], v[24:27]\n"
      "v_cvt_pk_bf16_f32 v50, v52, v53\n"
      "v_cvt_pk_bf16_f32 v51, v54, v55\n"
      "v_mfma_f32_16x16x32_bf16 v[64:67], a[108:111], a[56:59], v[64:67]\n"
      "buffer_store_dwordx4 v[48:51], v250, s[16:19], s44 offen offset:0 sc0 sc1\n"
      "v_mfma_f32_16x16x32_bf16 v[72:75], a[108:111], a[60:63], v[72:75]\n"
      "v_cvt_pk_bf16_f32 v56, v56, v57\n"
      "v_cvt_pk_bf16_f32 v57, v58, v59\n"
      "v_mfma_f32_16x16x32_bf16 v[20:23], a[112:115], a[48:51], v[20:23]\n"
      "v_cvt_pk_bf16_f32 v58, v60, v61\n"
      "v_cvt_pk_bf16_f32 v59, v62, v63\n"
      "v_mfma_f32_16x16x32_bf16 v[28:31], a[112:115], a[52:55], v[28:31]\n"
      "buffer_store_dwordx4 v[56:59], v250, s[16:19], s45 offen offset:0 sc0 sc1\n"
      "v_mfma_f32_16x16x32_bf16 v[68:71], a[112:115], a[56:59], v[68:71]\n"
      "v_mfma_f32_16x16x32_bf16 v[76:79], a[112:115], a[60:63], v[76:79]\n"
      "v_mfma_f32_16x16x32_bf16 v[16:19], a[140:143], a[64:67], v[16:19]\n"
      "v_mfma_f32_16x16x32_bf16 v[24:27], a[140:143], a[68:71], v[24:27]\n"
      "v_mfma_f32_16x16x32_bf16 v[64:67], a[140:143], a[72:75], v[64:67]\n"
      "ds_read_b64_tr_b16 a[24:25], v245 offset:0\n"
      "v_mfma_f32_16x16x32_bf16 v[72:75], a[140:143], a[76:79], v[72:75]\n"
      "ds_read_b64_tr_b16 a[26:27], v245 offset:3328\n"
      "v_mfma_f32_16x16x32_bf16 v[20:23], a[144:147], a[64:67], v[20:23]\n"
      "ds_read_b64_tr_b16 a[28:29], v245 offset:8\n"
      "v_mfma_f32_16x16x32_bf16 v[28:31], a[144:147], a[68:71], v[28:31]\n"
      "ds_read_b64_tr_b16 a[30:31], v245 offset:3336\n"
      "v_mfma_f32_16x16x32_bf16 v[68:71], a[144:147], a[72:75], v[68:71]\n"
      "ds_read_b64_tr_b16 a[124:125], v213 offset:0\n"
      "v_mfma_f32_16x16x32_bf16 v[76:79], a[144:147], a[76:79], v[76:79]\n"
      "ds_read_b64_tr_b16 a[126:127], v213 offset:3328\n"
      "v_mfma_f32_16x16x32_bf16 v[32:35], a[16:19], a[32:35], v[32:35]\n"
      "ds_read_b64_tr_b16 a[128:129], v213 offset:8\n"
      "v_mfma_f32_16x16x32_bf16 v[40:43], a[16:19], a[36:39], v[40:43]\n"
      "ds_read_b64_tr_b16 a[130:131], v213 offset:3336\n"
      "s_nop 2\n"
      "v_pk_mul_f32 v[16:17], v[16:17], v[120:121]\n"
      "v_pk_mul_f32 v[18:19], v[18:19], v[122:123]\n"
      "v_pk_mul_f32 v[20:21], v[20:21], v[124:125]\n"
      "v_pk_mul_f32 v[22:23], v[22:23], v[126:127]\n"
      "v_pk_mul_f32 v[24:25], v[24:25], v[128:129]\n"
      "v_pk_mul_f32 v[26:27], v[26:27], v[130:131]\n"
      "v_pk_mul_f32 v[28:29], v[28:29], v[132:133]\n"
      "v_pk_mul_f32 v[30:31], v[30:31], v[134:135]\n"
      "v_pk_mul_f32 v[64:65], v[64:65], v[168:169]\n"
      "v_pk_mul_f32 v[66:67], v[66:67], v[170:171]\n"
      "v_pk_mul_f32 v[68:69], v[68:69], v[172:173]\n"
      "v_pk_mul_f32 v[70:71], v[70:71], v[174:175]\n"
      "v_pk_mul_f32 v[72:73], v[72:73], v[176:177]\n"
      "v_pk_mul_f32 v[74:75], v[74:75], v[178:179]\n"
      "v_pk_mul_f32 v[76:77], v[76:77], v[180:181]\n"
      "v_pk_mul_f32 v[78:79], v[78:79], v[182:183]\n"
      "v_mfma_f32_16x16x32_bf16 v[36:39], a[20:23], a[32:35], v[36:39]\n"
      "v_cvt_pk_bf16_f32 v16, v16, v17\n"
      "v_cvt_pk_bf16_f32 v17, v18, v19\n"
      "v_mfma_f32_16x16x32_bf16 v[44:47], a[20:23], a[36:39], v[44:47]\n"
      "v_cvt_pk_bf16_f32 v18, v20, v21\n"
      "v_cvt_pk_bf16_f32 v19, v22, v23\n"
      "v_mfma_f32_16x16x32_bf16 v[32:35], a[116:119], a[48:51], v[32:35]\n"
      "buffer_store_dwordx4 v[16:19], v250, s[16:19], s42 offen offset:64 sc0 sc1\n"
      "v_mfma_f32_16x16x32_bf16 v[40:43], a[116:119], a[52:55], v[40:43]\n"
      "v_cvt_pk_bf16_f32 v24, v24, v25\n"
      "v_cvt_pk_bf16_f32 v25, v26, v27\n"
      "v_mfma_f32_16x16x32_bf16 v[36:39], a[120:123], a[48:51], v[36:39]\n"
      "v_cvt_pk_bf16_f32 v26, v28, v29\n"
      "v_cvt_pk_bf16_f32 v27, v30, v31\n"
      "v_mfma_f32_16x16x32_bf16 v[44:47], a[120:123], a[52:55], v[44:47]\n"
      "buffer_store_dwordx4 v[24:27], v250, s[16:19], s43 offen offset:64 sc0 sc1\n"
      "v_mfma_f32_16x16x32_bf16 v[32:35], a[148:151], a[64:67], v[32:35]\n"
      "v_cvt_pk_bf16_f32 v64, v64, v65\n"
      "v_cvt_pk_bf16_f32 v65, v66, v67\n"
      "v_mfma_f32_16x16x32_bf16 v[40:43], a[148:151], a[68:71], v[40:43]\n"
      "v_cvt_pk_bf16_f32 v66, v68, v69\n"
      "v_cvt_pk_bf16_f32 v67, v70, v71\n"
      "v_mfma_f32_16x16x32_bf16 v[36:39], a[152:155], a[64:67], v[36:39]\n"
      "ds_read_b64_tr_b16 a[156:157], v215 offset:0\n"
      "v_mfma_f32_16x16x32_bf16 v[44:47], a[152:155], a[68:71], v[44:47]\n"
      "ds_read_b64_tr_b16 a[158:159], v215 offset:3328\n"
      "v_mfma_f32_16x16x32_bf16 v[80:83], a[16:19], a[40:43], v[80:83]\n"
      "ds_read_b64_tr_b16 a[160:161], v215 offset:8\n"
      "v_mfma_f32_16x16x32_bf16 v[88:91], a[16:19], a[44:47], v[88:91]\n"
      "ds_read_b64_tr_b16 a[162:163], v215 offset:3336\n"
      "buffer_store_dwordx4 v[64:67], v250, s[16:19], s44 offen offset:64 sc0 sc1\n"
      "v_cvt_pk_bf16_f32 v72, v72, v73\n"
      "v_cvt_pk_bf16_f32 v73, v74, v75\n"
      "v_cvt_pk_bf16_f32 v74, v76, v77\n"
      "v_cvt_pk_bf16_f32 v75, v78, v79\n"
      "buffer_store_dwordx4 v[72:75], v250, s[16:19], s45 offen offset:64 sc0 sc1\n"
      "s_nop 2\n"
      "v_pk_mul_f32 v[32:33], v[32:33], v[136:137]\n"
      "v_pk_mul_f32 v[34:35], v[34:35], v[138:139]\n"
      "v_pk_mul_f32 v[36:37], v[36:37], v[140:141]\n"
      "v_pk_mul_f32 v[38:39], v[38:39], v[142:143]\n"
      "v_pk_mul_f32 v[40:41], v[40:41], v[144:145]\n"
      "v_pk_mul_f32 v[42:43], v[42:43], v[146:147]\n"
      "v_pk_mul_f32 v[44:45], v[44:45], v[148:149]\n"
      "v_pk_mul_f32 v[46:47], v[46:47], v[150:151]\n"
      "v_mfma_f32_16x16x32_bf16 v[84:87], a[20:23], a[40:43], v[84:87]\n"
      "v_cvt_pk_bf16_f32 v32, v32, v33\n"
      "v_cvt_pk_bf16_f32 v33, v34, v35\n"
      "v_mfma_f32_16x16x32_bf16 v[92:95], a[20:23], a[44:47], v[92:95]\n"
      "v_cvt_pk_bf16_f32 v34, v36, v37\n"
      "v_cvt_pk_bf16_f32 v35, v38, v39\n"
      "v_mfma_f32_16x16x32_bf16 v[80:83], a[116:119], a[56:59], v[80:83]\n"
      "buffer_store_dwordx4 v[32:35], v250, s[16:19], s42 offen offset:128 sc0 sc1\n"
      "v_mfma_f32_16x16x32_bf16 v[88:91], a[116:119], a[60:63], v[88:91]\n"
      "v_cvt_pk_bf16_f32 v40, v40, v41\n"
      "v_cvt_pk_bf16_f32 v41, v42, v43\n"
      "v_mfma_f32_16x16x32_bf16 v[84:87], a[120:123], a[56:59], v[84:87]\n"
      "v_cvt_pk_bf16_f32 v42, v44, v45\n"
      "v_cvt_pk_bf16_f32 v43, v46, v47\n"
      "v_mfma_f32_16x16x32_bf16 v[92:95], a[120:123], a[60:63], v[92:95]\n"
      "buffer_store_dwordx4 v[40:43], v250, s[16:19], s43 offen offset:128 sc0 sc1\n"
      "v_mfma_f32_16x16x32_bf16 v[80:83], a[148:151], a[72:75], v[80:83]\n"
      "v_mfma_f32_16x16x32_bf16 v[88:91], a[148:151], a[76:79], v[88:91]\n"
      "v_mfma_f32_16x16x32_bf16 v[84:87], a[152:155], a[72:75], v[84:87]\n"
      "v_mfma_f32_16x16x32_bf16 v[92:95], a[152:155], a[76:79], v[92:95]\n"
      "s_waitcnt lgkmcnt(0)\n"
      "v_mfma_f32_16x16x32_bf16 v[96:99], a[24:27], a[32:35], v[96:99]\n"
      "v_mfma_f32_16x16x32_bf16 v[100:103], a[28:31], a[32:35], v[100:103]\n"
      "s_nop 2\n"
      "v_pk_mul_f32 v[80:81], v[80:81], v[184:185]\n"
      "v_pk_mul_f32 v[82:83], v[82:83], v[186:187]\n"
      "v_pk_mul_f32 v[84:85], v[84:85], v[188:189]\n"
      "v_pk_mul_f32 v[86:87], v[86:87], v[190:191]\n"
      "v_pk_mul_f32 v[88:89], v[88:89], v[192:193]\n"
      "v_pk_mul_f32 v[90:91], v[90:91], v[194:195]\n"
      "v_pk_mul_f32 v[92:93], v[92:93], v[196:197]\n"
      "v_pk_mul_f32 v[94:95], v[94:95], v[198:199]\n"
      "v_mfma_f32_16x16x32_bf16 v[96:99], a[124:127], a[48:51], v[96:99]\n"
      "v_cvt_pk_bf16_f32 v80, v80, v81\n"
      "v_cvt_pk_bf16_f32 v81, v82, v83\n"
      "v_mfma_f32_16x16x32_bf16 v[100:103], a[128:131], a[48:51], v[100:103]\n"
      "v_cvt_pk_bf16_f32 v82, v84, v85\n"
      "v_cvt_pk_bf16_f32 v83, v86, v87\n"
      "v_mfma_f32_16x16x32_bf16 v[96:99], a[156:159], a[64:67], v[96:99]\n"
      "buffer_store_dwordx4 v[80:83], v250, s[16:19], s44 offen offset:128 sc0 sc1\n"
      "v_mfma_f32_16x16x32_bf16 v[100:103], a[160:163], a[64:67], v[100:103]\n"
      "v_cvt_pk_bf16_f32 v88, v88, v89\n"
      "v_cvt_pk_bf16_f32 v89, v90, v91\n"
      "v_cvt_pk_bf16_f32 v90, v92, v93\n"
      "v_cvt_pk_bf16_f32 v91, v94, v95\n"
      "buffer_store_dwordx4 v[88:91], v250, s[16:19], s45 offen offset:128 sc0 sc1\n"
      "s_nop 3\n"
      "v_pk_mul_f32 v[96:97], v[96:97], v[200:201]\n"
      "v_pk_mul_f32 v[98:99], v[98:99], v[202:203]\n"
      "v_pk_mul_f32 v[100:101], v[100:101], v[204:205]\n"
      "v_pk_mul_f32 v[102:103], v[102:103], v[206:207]\n"
      "v_cvt_pk_bf16_f32 v96, v96, v97\n"
      "v_cvt_pk_bf16_f32 v97, v98, v99\n"
      "v_cvt_pk_bf16_f32 v98, v100, v101\n"
      "v_cvt_pk_bf16_f32 v99, v102, v103\n"
      "buffer_store_dwordx4 v[96:99], v251, s[16:19], s42 offen offset:768 sc0 sc1\n"
      "s_waitcnt vmcnt(0)\n"
      "LCEND_%=:\n"
      :
      : [tid] "v"(tid), [bid] "s"(bid), [lds] "s"(lds0), [xp] "s"(x0g), [wp] "s"(wg), [bp] "s"(bg), [op] "s"(outg)
      :
      "v0", "v1", "v2", "v3", "v4", "v5", "v6", "v7", "v8", "v9", "v10", "v11", "v12", "v13", "v14", "v15",
      "v16", "v17", "v18", "v19", "v20", "v21", "v22", "v23", "v24", "v25", "v26", "v27", "v28", "v29", "v30",
      "v31", "v32", "v33", "v34", "v35", "v36", "v37", "v38", "v39", "v40", "v41", "v42", "v43", "v44", "v45",
      "v46", "v47", "v48", "v49", "v50", "v51", "v52", "v53", "v54", "v55", "v56", "v57", "v58", "v59", "v60",
      "v61", "v62", "v63", "v64", "v65", "v66", "v67", "v68", "v69", "v70", "v71", "v72", "v73", "v74", "v75",
      "v76", "v77", "v78", "v79", "v80", "v81", "v82", "v83", "v84", "v85", "v86", "v87", "v88", "v89", "v90",
      "v91", "v92", "v93", "v94", "v95", "v96", "v97", "v98", "v99", "v100", "v101", "v102", "v103", "v104",
      "v105", "v106", "v107", "v108", "v109", "v110", "v111", "v112", "v113", "v114", "v115", "v116", "v117",
      "v118", "v119", "v120", "v121", "v122", "v123", "v124", "v125", "v126", "v127", "v128", "v129", "v130",
      "v131", "v132", "v133", "v134", "v135", "v136", "v137", "v138", "v139", "v140", "v141", "v142", "v143",
      "v144", "v145", "v146", "v147", "v148", "v149", "v150", "v151", "v152", "v153", "v154", "v155", "v156",
      "v157", "v158", "v159", "v160", "v161", "v162", "v163", "v164", "v165", "v166", "v167", "v168", "v169",
      "v170", "v171", "v172", "v173", "v174", "v175", "v176", "v177", "v178", "v179", "v180", "v181", "v182",
      "v183", "v184", "v185", "v186", "v187", "v188", "v189", "v190", "v191", "v192", "v193", "v194", "v195",
      "v196", "v197", "v198", "v199", "v200", "v201", "v202", "v203", "v204", "v205", "v206", "v207", "v208",
      "v209", "v210", "v211", "v212", "v213", "v214", "v215", "v216", "v217", "v218", "v219", "v220", "v221",
      "v222", "v223", "v224", "v225", "v226", "v227", "v228", "v229", "v230", "v231", "v232", "v233", "v234",
      "v235", "v236", "v237", "v238", "v239", "v240", "v241", "v242", "v243", "v244", "v245", "v246", "v247",
      "v248", "v249", "v250", "v251", "v252", "v253", "v254", "a0", "a1", "a2", "a3", "a4", "a5", "a6", "a7",
      "a8", "a9", "a10", "a11", "a12", "a13", "a14", "a15", "a16", "a17", "a18", "a19", "a20", "a21", "a22",
      "a23", "a24", "a25", "a26", "a27", "a28", "a29", "a30", "a31", "a32", "a33", "a34", "a35", "a36", "a37",
      "a38", "a39", "a40", "a41", "a42", "a43", "a44", "a45", "a46", "a47", "a48", "a49", "a50", "a51", "a52",
      "a53", "a54", "a55", "a56", "a57", "a58", "a59", "a60", "a61", "a62", "a63", "a64", "a65", "a66", "a67",
      "a68", "a69", "a70", "a71", "a72", "a73", "a74", "a75", "a76", "a77", "a78", "a79", "a80", "a81", "a82",
      "a83", "a84", "a85", "a86", "a87", "a88", "a89", "a90", "a91", "a92", "a93", "a94", "a95", "a96", "a97",
      "a98", "a99", "a100", "a101", "a102", "a103", "a104", "a105", "a106", "a107", "a108", "a109", "a110",
      "a111", "a112", "a113", "a114", "a115", "a116", "a117", "a118", "a119", "a120", "a121", "a122", "a123",
      "a124", "a125", "a126", "a127", "a128", "a129", "a130", "a131", "a132", "a133", "a134", "a135", "a136",
      "a137", "a138", "a139", "a140", "a141", "a142", "a143", "a144", "a145", "a146", "a147", "a148", "a149",
      "a150", "a151", "a152", "a153", "a154", "a155", "a156", "a157", "a158", "a159", "a160", "a161", "a162",
      "a163", "s8", "s9", "s10", "s11", "s12", "s13", "s14", "s15", "s16", "s17", "s18", "s19", "s20", "s21",
      "s22", "s23", "s24", "s25", "s26", "s27", "s28", "s29", "s30", "s31", "s32", "s33", "s34", "s35", "s36",
      "s37", "s38", "s39", "s40", "s41", "s42", "s43", "s44", "s45", "s46", "s47", "s48", "s49", "s50", "s51",
      "s52", "s53", "s54", "s55", "s56", "s57", "s58", "s59", "s60", "s61", "s62", "s63", "vcc", "scc",
      "memory");
}

extern "C" void kernel_launch(void* const* d_in, const int* in_sizes, int n_in, void* d_out, int out_size,
                              void* d_ws, size_t ws_size, hipStream_t stream) {
  const __hip_bfloat16* x0 = (const __hip_bfloat16*)d_in[0];
  const __hip_bfloat16* ws = (const __hip_bfloat16*)d_in[1];
  const __hip_bfloat16* bs = (const __hip_bfloat16*)d_in[2];
  __hip_bfloat16* out = (__hip_bfloat16*)d_out;
  const int B = in_sizes[0] / 416;
  dcn_fused_kernel<<<B / 64, 256, 0, stream>>>(x0, ws, bs, out);
}
```
